```python
import torch

def _softmax(x, axis=-1):
    return torch.softmax(x, dim=axis)
def _gelu(x):
    return torch.nn.functional.gelu(x, approximate="tanh")
def _silu(x):
    return torch.nn.functional.silu(x)
def _sigmoid(x):
    return torch.sigmoid(x)
def _layernorm(x, axis=-1, eps=1e-5):
    m = x.mean(dim=axis, keepdim=True)
    v = x.var(dim=axis, keepdim=True, unbiased=False)
    return (x - m) / torch.sqrt(v + eps)
def _scatter_add(tgt, idx, src, axis=0):
    return tgt.clone().index_add_(axis, idx, src)
def _segment_sum(src, seg, n):
    out = torch.zeros((n,) + src.shape[1:], dtype=src.dtype)
    return out.index_add_(0, seg, src)
def _pad(x, pw):
    flat = tuple(v for pair in reversed(pw) for v in pair)
    return torch.nn.functional.pad(x, flat)

kernel_name = 'sequential_preLN__gqa_block_cached__geglu_ffn__decode'

def setup_inputs():

    _w = (lambda a: (lambda t: t.to(torch.bfloat16) if t.is_floating_point() else t)(torch.as_tensor(a)))
    def _A_setup():
        B, Sq, D = 4, 1, 512
        Hq, Hkv, Sk, Dh = 32, 8, 16384, 128
        assert Hq % Hkv == 0, (Hq, Hkv)
        return {
            "x": _w((lambda *s: torch.randn(*s, dtype=torch.bfloat16) * 0.05)(B, Sq, D)),
            "k_cache": _w((lambda *s: torch.randn(*s, dtype=torch.bfloat16) * 3.2)(B, Hkv, Sk, Dh)),
            "v_cache": _w((lambda *s: torch.randn(*s, dtype=torch.bfloat16) * 0.4)(B, Hkv, Sk, Dh)),
            "wq": _w((lambda *s: torch.randn(*s, dtype=torch.bfloat16) * 3.2)(D, Hq * Dh) * (D ** -0.5)),
            "wo": _w((lambda *s: torch.randn(*s, dtype=torch.bfloat16) * 0.4)(Hq * Dh, D) * ((Hq * Dh) ** -0.5)),
        }
    _A_in = _A_setup()

    def _B_setup():
        B, S, D, F = 4, 1, 512, 1792
        return {
            "x": _w((lambda *s: torch.randn(*s, dtype=torch.bfloat16) * 0.05)(B, S, D)),
            "w_gate": _w((lambda *s: torch.randn(*s, dtype=torch.bfloat16) * 0.05)(D, F)),
            "w_up": _w((lambda *s: torch.randn(*s, dtype=torch.bfloat16) * 0.05)(D, F)),
            "w_down": _w((lambda *s: torch.randn(*s, dtype=torch.bfloat16) * 0.05)(F, D)),
        }
    _B_in = _B_setup()

    return {
        "A_x": _A_in["x"],
        "A_k_cache": _A_in["k_cache"],
        "A_v_cache": _A_in["v_cache"],
        "A_wq": _A_in["wq"],
        "A_wo": _A_in["wo"],
        "B_x": _B_in["x"],
        "B_w_gate": _B_in["w_gate"],
        "B_w_up": _B_in["w_up"],
        "B_w_down": _B_in["w_down"],
    }

def reference(A_x, A_k_cache, A_v_cache, A_wq, A_wo, B_x, B_w_gate, B_w_up, B_w_down):
    def _A_ref(x, k_cache, v_cache, wq, wo):
        B, Sq, D = x.shape
        Hq, Dh = 32, 128
        Hkv = k_cache.shape[1]
        rep = Hq // Hkv
        q = (lambda x, *d: x.permute(*d))((x @ wq).reshape(B, Sq, Hq, Dh), 0, 2, 1, 3)
        k = (lambda x, n, axis: torch.repeat_interleave(x, n, dim=axis))(k_cache, rep, axis=1)
        v = (lambda x, n, axis: torch.repeat_interleave(x, n, dim=axis))(v_cache, rep, axis=1)
        scores = torch.einsum("bhqd,bhkd->bhqk", q, k) * (Dh ** -0.5)
        p = _softmax(scores, axis=-1)
        o = torch.einsum("bhqk,bhkd->bhqd", p, v)
        return (lambda x, *d: x.permute(*d))(o, 0, 2, 1, 3).reshape(B, Sq, Hq * Dh) @ wo

    def _B_ref(x, w_gate, w_up, w_down):
        return (_gelu(x @ w_gate) * (x @ w_up)) @ w_down

    x = A_x
    h = x + _A_ref(_layernorm(x, axis=-1), A_k_cache, A_v_cache, A_wq, A_wo)
    h2 = h + _B_ref(_layernorm(h, axis=-1), B_w_gate, B_w_up, B_w_down)
    return h2
```

```cpp
#define EARLYB 1
#define TSENT 1
#include <hip/hip_runtime.h>
#include <hip/hip_bf16.h>
#include <cstdint>

typedef unsigned u32x4 __attribute__((ext_vector_type(4)));
typedef unsigned u32x2 __attribute__((ext_vector_type(2)));
typedef float f32x4 __attribute__((ext_vector_type(4)));
typedef float f32x2 __attribute__((ext_vector_type(2)));
typedef short bf16x8 __attribute__((ext_vector_type(8)));
typedef short bf16x4 __attribute__((ext_vector_type(4)));
#define LAS __attribute__((address_space(3)))
#define GAS __attribute__((address_space(1)))
#define CAS __attribute__((address_space(4)))
typedef __amdgpu_buffer_rsrc_t rsrc_t;

#define NP 6
#define ROT 37
#define QSLEEP 40
#define QSLEEP2 10
#define SLANES 64
#define CREP 4
#define GWAIT 1
#define GU_NT ""

constexpr int SK = 16384, DH = 128, FF = 1792;
constexpr int AUX_NT = 2, AUX_SC1 = 16, RSRC3 = 0x00020000;
constexpr unsigned SPIN_CAP = 1u << 16;
constexpr int OOB = 0x7ffffff0;

constexpr int WS_E = 0;
constexpr int WS_Q = 4096;
constexpr int WS_A = WS_Q + 16384 * 8;
constexpr int WS_ML = WS_A + 131072 * 4;
constexpr int WS_B = WS_ML + 1024 * 16;
constexpr int WS_C = WS_B + 131072 * 4;
constexpr int WS_D = WS_C + 4 * 2048 * 4;
constexpr int WS_END = WS_D + 131072 * 4;
constexpr int WS_ST = WS_END;

constexpr int L_W = 0;
constexpr int L_WH = 24576;
constexpr int L_ACT = 4 * L_WH;
constexpr int APITCH = 1040;
constexpr int L_RED = L_ACT + 4 * APITCH;
constexpr int L_ML = L_RED + 8192;
constexpr int L_AI = L_ML + 128;
constexpr int L_QI = L_AI + 1024;
constexpr int L_RED2 = L_QI + 2048;
constexpr int L_QF = L_RED2 + 4096;
constexpr int L_FLAG = L_QF + 32;
constexpr int L_END = L_FLAG + 16;

__device__ __forceinline__ unsigned cvtpk(float lo, float hi) {
  typedef __bf16 bf2_t __attribute__((ext_vector_type(2)));
  f32x2 v = {lo, hi}; return __builtin_bit_cast(unsigned, __builtin_convertvector(v, bf2_t));
}
__device__ __forceinline__ float bflo(unsigned u) { return __builtin_bit_cast(float, u << 16); }
__device__ __forceinline__ float bfhi(unsigned u) { return __builtin_bit_cast(float, u & 0xffff0000u); }
__device__ __forceinline__ float asf(unsigned u) { return __builtin_bit_cast(float, u); }
__device__ __forceinline__ unsigned asu(float f) { return __builtin_bit_cast(unsigned, f); }
template <int CTRL> __device__ __forceinline__ float dpp(float x) {
  return __builtin_bit_cast(float, __builtin_amdgcn_mov_dpp(__builtin_bit_cast(int, x), CTRL, 0xf, 0xf, true));
}
__device__ __forceinline__ float xg_max(float x) {
  auto s = __builtin_amdgcn_permlane16_swap(asu(x), asu(x), false, false);
  x = fmaxf(asf(s[0]), asf(s[1]));
  auto t = __builtin_amdgcn_permlane32_swap(asu(x), asu(x), false, false);
  return fmaxf(asf(t[0]), asf(t[1]));
}
__device__ __forceinline__ float tr16(float a, float b) { auto s = __builtin_amdgcn_permlane16_swap(asu(a), asu(b), false, false); return asf(s[0]) + asf(s[1]); }
__device__ __forceinline__ float tr32(float a, float b) { auto s = __builtin_amdgcn_permlane32_swap(asu(a), asu(b), false, false); return asf(s[0]) + asf(s[1]); }
__device__ __forceinline__ float xg_sum(float x) { x = tr16(x, x); return tr32(x, x); }
__device__ __forceinline__ float sum16(float x) { x += dpp<0x124>(x); x += dpp<0x128>(x); return xg_sum(x); }
__device__ __forceinline__ float wave_sum(float x) { x += dpp<0xB1>(x); x += dpp<0x4E>(x); return sum16(x); }
#define BARRIER() do { __builtin_amdgcn_sched_barrier(0); asm volatile("s_waitcnt lgkmcnt(0)\n\ts_barrier" ::: "memory"); __builtin_amdgcn_sched_barrier(0); } while (0)
#define LGKM0() asm volatile("s_waitcnt lgkmcnt(0)" ::: "memory")
#define VM0() asm volatile("s_waitcnt vmcnt(0)" ::: "memory")

__device__ __forceinline__ u32x4 ldpoll(rsrc_t rs, int off) {
  asm volatile("" : "+v"(off) :: "memory");
  return __builtin_amdgcn_raw_buffer_load_b128(rs, off, 0, AUX_SC1);
}
__device__ __forceinline__ unsigned ldsu(LAS unsigned char* p) { return __builtin_amdgcn_readfirstlane(*(volatile LAS unsigned*)p); }
struct Tag { unsigned t[4]; };
__device__ __forceinline__ unsigned enc(float v, unsigned t) { return ((asu(v) + 0x800u) & 0xfffff000u) | t; }
__device__ __forceinline__ float dec(unsigned u) { return asf(u & 0xfffff000u); }
__device__ __forceinline__ f32x4 dec4(u32x4 v) { return f32x4{dec(v[0]), dec(v[1]), dec(v[2]), dec(v[3])}; }
__device__ __forceinline__ void st4(rsrc_t rs, int off, f32x4 v, const Tag& g) {
  __builtin_amdgcn_raw_buffer_store_b128(u32x4{enc(v[0], g.t[0]), enc(v[1], g.t[1]), enc(v[2], g.t[2]), enc(v[3], g.t[3])}, rs, off, 0, AUX_SC1);
}
__device__ __forceinline__ unsigned bad4(u32x4 v, const Tag& g) { return (v[0] ^ g.t[0]) | (v[1] ^ g.t[1]) | (v[2] ^ g.t[2]) | (v[3] ^ g.t[3]); }
__device__ __forceinline__ Tag make_tag(unsigned long long e) {
  Tag g{{(unsigned)e & 0xfffu, (unsigned)(e >> 12) & 0xfffu, (unsigned)(e >> 24) & 0xfffu, (unsigned)(e >> 36) & 0xfffu}};
  asm volatile("" : "+v"(g.t[0]), "+v"(g.t[1]), "+v"(g.t[2]), "+v"(g.t[3]));
  return g;
}
#define PIN(x) asm volatile("" : "+v"(x))
template <int N, int SENT> __device__ __forceinline__ void gather(rsrc_t rs, const int (&off)[N], u32x4 (&v)[N], const Tag& g, int lane) {
  if (SENT) {
    const int o0 = lane < SLANES ? off[0] : OOB;
#pragma nounroll
    for (unsigned spins = 0; spins < SPIN_CAP; ++spins) {
      const u32x4 s0 = ldpoll(rs, o0);
      if (__any((bad4(s0, g) & 0xfffu) == 0u && lane < SLANES)) break;
      __builtin_amdgcn_s_sleep(SENT);
    }
  }
  unsigned bad = 1u;
#pragma unroll
  for (int i = 0; i < N; ++i) v[i] = u32x4{0u, 0u, 0u, 0u};
#pragma nounroll
  for (unsigned spins = 0; spins < SPIN_CAP; ++spins) {
    if (bad) {
#pragma unroll
      for (int i = 0; i < N; ++i) v[i] = ldpoll(rs, off[i]);
      bad = 0u;
#pragma unroll
      for (int i = 0; i < N; ++i) bad |= bad4(v[i], g);
      bad &= 0xfffu;
    }
    if (__all(bad == 0u)) break;
  }
  asm volatile("" ::: "memory");
}
#define DMA16(NAME, POL) \
template <int LOFF, int SOFF> __device__ __forceinline__ void NAME(rsrc_t rs, unsigned lds, int voff) { \
  unsigned keep, so; \
  asm volatile("s_mov_b32 %0, m0\n\ts_add_u32 m0, %3, %5\n\ts_mov_b32 %1, %6\n\tbuffer_load_dwordx4 %2, %4, %1 offen " POL "lds\n\ts_mov_b32 m0, %0" \
               : "=&s"(keep), "=&s"(so) : "v"(voff), "s"(lds), "s"(rs), "i"(LOFF), "i"(SOFF) : "memory", "scc"); \
} \
template <int I, int N, int L0, int SSTR> __device__ __forceinline__ void NAME##_run(rsrc_t rs, unsigned lds, int voff) { \
  if constexpr (I < N) { NAME<L0 + I * 1024, I * SSTR>(rs, lds, voff); NAME##_run<I + 1, N, L0, SSTR>(rs, lds, voff); } \
}
DMA16(dma, "nt ")
DMA16(dmag, GU_NT)
__device__ __forceinline__ bf16x8 trfrag(LAS unsigned char* p, int second) {
  typedef short s4 __attribute__((ext_vector_type(4)));
  const s4 a = __builtin_amdgcn_ds_read_tr16_b64_v4i16((LAS s4*)p);
  const s4 b = __builtin_amdgcn_ds_read_tr16_b64_v4i16((LAS s4*)(p + second));
  return __builtin_shufflevector(a, b, 0, 1, 2, 3, 4, 5, 6, 7);
}
__device__ __forceinline__ f32x4 mfma32(bf16x8 a, bf16x8 b, f32x4 c) { return __builtin_amdgcn_mfma_f32_16x16x32_bf16(a, b, c, 0, 0, 0); }
__device__ __forceinline__ f32x4 pick4(f32x4 a0, f32x4 a1, f32x4 a2, f32x4 a3, int n) {
  f32x4 r;
#pragma unroll
  for (int i = 0; i < 4; ++i) r[i] = (n & 8) ? ((n & 4) ? a3[i] : a2[i]) : ((n & 4) ? a1[i] : a0[i]);
  return r;
}

#define STAMP(i) do {} while (0)
#define EXIT() return

struct St { f32x4 olo, ohi; float m, l; };
template <bool REFILL>
__device__ __forceinline__ void step(u32x4 (&K)[4], u32x4 (&V)[4], const bf16x8 (&qf)[4], St& s, rsrc_t rk, rsrc_t rv, int koff, int voff, int noff, unsigned M0, unsigned M1) {
  f32x4 S = {0.f, 0.f, 0.f, 0.f};
#pragma unroll
  for (int r = 0; r < 4; ++r) {
    S = mfma32(__builtin_bit_cast(bf16x8, K[r]), qf[r], S);
    __builtin_amdgcn_sched_barrier(0);
    if (REFILL) K[r] = __builtin_amdgcn_raw_buffer_load_b128(rk, koff + r * 64, noff, AUX_NT);
    __builtin_amdgcn_sched_barrier(0);
  }
#pragma unroll
  for (int i = 0; i < 4; ++i) S[i] += dpp<0xB1>(S[i]);
  float mc = fmaxf(fmaxf(S[0], S[1]), fmaxf(S[2], S[3]));
  if (__any(mc > s.m)) {
    mc = fmaxf(xg_max(mc), s.m);
    const float al = __builtin_amdgcn_exp2f(s.m - mc);
    s.l *= al; s.olo *= al; s.ohi *= al; s.m = mc;
  }
#pragma unroll
  for (int r = 0; r < 4; ++r) {
    const float p = __builtin_amdgcn_exp2f(S[r] - s.m);
    const unsigned pk = cvtpk(p, p);
    s.l += bfhi(pk);
    const u32x2 bb = {pk & M0, pk & M1};
    const u32x2 alo = {V[r][0], V[r][1]}, ahi = {V[r][2], V[r][3]};
    s.olo = __builtin_amdgcn_mfma_f32_16x16x16bf16_1k(__builtin_bit_cast(bf16x4, alo), __builtin_bit_cast(bf16x4, bb), s.olo, 0, 0, 0);
    s.ohi = __builtin_amdgcn_mfma_f32_16x16x16bf16_1k(__builtin_bit_cast(bf16x4, ahi), __builtin_bit_cast(bf16x4, bb), s.ohi, 0, 0, 0);
    __builtin_amdgcn_sched_barrier(0);
    if (REFILL) V[r] = __builtin_amdgcn_raw_buffer_load_b128(rv, voff + r * 1024, noff, AUX_NT);
    __builtin_amdgcn_sched_barrier(0);
  }
}

__global__ void __launch_bounds__(512, 2) fused_decode_block(const unsigned short* __restrict__ x, const unsigned short* __restrict__ kc, const unsigned short* __restrict__ vc,
                                                             const unsigned short* __restrict__ wq, const unsigned short* __restrict__ wo, const unsigned short* __restrict__ wg,
                                                             const unsigned short* __restrict__ wu, const unsigned short* __restrict__ wd, unsigned short* out, unsigned char* ws) {
  extern __shared__ __attribute__((aligned(16))) unsigned char smem[];
  LAS unsigned char* L = (LAS unsigned char*)smem;
  const int tid = threadIdx.x, lane = tid & 63, w = __builtin_amdgcn_readfirstlane(tid >> 6), blk = blockIdx.x;
  const int g = lane >> 4, n = lane & 15;
  const int pair = blk >> 3, split = blk & 7;
  const int b = pair >> 3, hkv = pair & 7;
  const int ri = (split + pair) & 7;
  const int ridx = ri < 4 ? pair * 2 + (ri & 1) : ri == 4 ? pair * 2 : ri == 5 ? ((pair + 31) & 31) * 2 + 1 : pair;
  const int role = ri < 4 ? 0 : ri < 6 ? (ridx < 56 ? 1 : 3) : ri == 6 ? 2 : 3;
  const rsrc_t rw = __builtin_amdgcn_make_buffer_rsrc((void*)ws, 0, WS_END, RSRC3);

  if (w < 4) {
    const rsrc_t rk = __builtin_amdgcn_make_buffer_rsrc((void*)(kc + (size_t)pair * SK * DH), 0, SK * 256, RSRC3);
    const rsrc_t rv = __builtin_amdgcn_make_buffer_rsrc((void*)(vc + (size_t)pair * SK * DH), 0, SK * 256, RSRC3);
    const int koff = (4 * (n & 3) + (n >> 2)) * 256 + 16 * g, voff = lane * 16;
    const int t0 = split * 4 + w + pair * ROT;
#define TOFF(t) ((((t) * 32 + t0) & 1023) * 4096)
    u32x4 K[NP][4], V[NP][4];
#pragma unroll
    for (int t = 0; t < NP; ++t) {
#pragma unroll
      for (int r = 0; r < 4; ++r) K[t][r] = __builtin_amdgcn_raw_buffer_load_b128(rk, koff + r * 64, TOFF(t), AUX_NT);
      if (EARLYB && t == 0) BARRIER();
#pragma unroll
      for (int r = 0; r < 4; ++r) V[t][r] = __builtin_amdgcn_raw_buffer_load_b128(rv, voff + r * 1024, TOFF(t), AUX_NT);
      __builtin_amdgcn_sched_barrier(0);
    }
    STAMP(1);
    BARRIER();
    STAMP(2);
    bf16x8 qf[4];
#pragma unroll
    for (int j = 0; j < 4; ++j) qf[j] = *(LAS bf16x8*)(L + L_QI + (n & 1) * 1024 + (n >> 2) * 256 + j * 64 + g * 16);
    const unsigned M0 = (n & 2) ? 0u : ((n & 1) ? 0xffff0000u : 0xffffu), M1 = (n & 2) ? ((n & 1) ? 0xffff0000u : 0xffffu) : 0u;
    St s; s.olo = f32x4{0.f, 0.f, 0.f, 0.f}; s.ohi = s.olo; s.m = -3.0e38f; s.l = 0.f;
    step<true>(K[0], V[0], qf, s, rk, rv, koff, voff, TOFF(NP), M0, M1);
    step<true>(K[1], V[1], qf, s, rk, rv, koff, voff, TOFF(NP + 1), M0, M1);
#pragma unroll
    for (int t = 2; t < NP; ++t) step<false>(K[t], V[t], qf, s, rk, rv, koff, voff, 0, M0, M1);
    const Tag tag = make_tag(((unsigned long long)ldsu(L + L_QF + 4) << 32) | ldsu(L + L_QF));
    for (int r = NP; r < 30; r += 2) {
      step<true>(K[0], V[0], qf, s, rk, rv, koff, voff, TOFF(r + 2), M0, M1);
      step<true>(K[1], V[1], qf, s, rk, rv, koff, voff, TOFF(r + 3), M0, M1);
    }
    if (tid == 0) *(volatile LAS unsigned*)(L + L_FLAG + 4) = 1u;
    step<false>(K[0], V[0], qf, s, rk, rv, koff, voff, 0, M0, M1);
    step<false>(K[1], V[1], qf, s, rk, rv, koff, voff, 0, M0, M1);
    STAMP(3);
    *(LAS f32x4*)(L + L_RED + (w * 64 + lane) * 32) = s.olo;
    *(LAS f32x4*)(L + L_RED + (w * 64 + lane) * 32 + 16) = s.ohi;
    s.l = xg_sum(s.l);
    if (lane < 16 && (n & 3) == 0) *(LAS f32x2*)(L + L_ML + ((n >> 2) * 4 + w) * 8) = f32x2{s.m, s.l};
    LGKM0();
    if (lane == 0) __hip_atomic_fetch_add((LAS unsigned*)(L + L_FLAG), 1u, __ATOMIC_RELAXED, __HIP_MEMORY_SCOPE_WORKGROUP);
    const int hq = hkv * 4 + w, j = lane & 31;
    int poff = lane < 32 ? WS_A + ((((hq * 2 + (j >> 4)) * 4 + b) * 8 + split) * 64 + (j & 15) * 4) * 4 : lane == 32 ? WS_ML + ((hq * 4 + b) * 8 + split) * 16 : OOB;
    int src = L_RED + ((j >> 3) * 16 + w * 4 + ((j >> 1) & 3)) * 32 + (j & 1) * 16;
    PIN(poff); PIN(src);
#pragma nounroll
    for (unsigned spins = 0; spins < (SPIN_CAP << 6); ++spins) { if (ldsu(L + L_FLAG) == 4u) break; }
    asm volatile("" ::: "memory");
    STAMP(13);
    {
      const f32x4 ml0 = *(const LAS f32x4*)(L + L_ML + w * 32), ml1 = *(const LAS f32x4*)(L + L_ML + w * 32 + 16);
      f32x4 p[4];
#pragma unroll
      for (int i = 0; i < 4; ++i) p[i] = *(const LAS f32x4*)(L + src + i * 2048);
      const float M = dec(enc(fmaxf(fmaxf(ml0[0], ml0[2]), fmaxf(ml1[0], ml1[2])), 0u));
      const float s0 = __builtin_amdgcn_exp2f(ml0[0] - M), s1 = __builtin_amdgcn_exp2f(ml0[2] - M), s2 = __builtin_amdgcn_exp2f(ml1[0] - M), s3 = __builtin_amdgcn_exp2f(ml1[2] - M);
      f32x4 o = (s0 * p[0] + s1 * p[1]) + (s2 * p[2] + s3 * p[3]);
      const float Ls = (s0 * ml0[1] + s1 * ml0[3]) + (s2 * ml1[1] + s3 * ml1[3]);
      if (lane >= 32) o = f32x4{M, Ls, M, Ls};
      st4(rw, poff, o, tag);
    }
    STAMP(4);
    if (role == 0) {
      const int c = ridx, sp = lane >> 3, p0 = (lane & 7) * 8;
      const int o0 = WS_A + (((c * 4 + w) * 8 + sp) * 64 + p0) * 4;
      const int off[3] = {o0, o0 + 16, WS_ML + (((c >> 1) * 4 + w) * 8 + sp) * 16};
      int ao = L_ACT + w * APITCH + (p0 + 4 * (g & 1) + 2 * (g >> 1)) * 2;
      PIN(ao);
      u32x4 v[3];
      gather<3, 0>(rw, off, v, tag, lane);
      STAMP(8);
      const float Ms = dec(v[2][0]);
      float M = fmaxf(Ms, dpp<0x128>(Ms)); M = xg_max(M);
      const float sc = __builtin_amdgcn_exp2f(Ms - M);
      float Ls = dec(v[2][1]) * sc; Ls += dpp<0x128>(Ls); Ls = xg_sum(Ls);
      const float f = sc * __builtin_amdgcn_rcpf(Ls);
      float a[4];
#pragma unroll
      for (int i = 0; i < 4; ++i) a[i] = tr16(dec(v[0][i]) * f, dec(v[1][i]) * f);
      float e0 = tr32(a[0], a[2]), e1 = tr32(a[1], a[3]);
      e0 += dpp<0x128>(e0); e1 += dpp<0x128>(e1);
      *(LAS unsigned*)(L + ao) = cvtpk(e0, e1);
      BARRIER();
    } else if (role == 1) {
      const int o0 = WS_C + ((ridx & (CREP - 1)) * 2048 + w * 512 + lane * 4) * 4;
      const int off[2] = {o0, o0 + 1024};
      u32x4 v[2];
      gather<2, TSENT>(rw, off, v, tag, lane);
      STAMP(8);
      const float piv = dec(__builtin_amdgcn_readfirstlane(v[0][0]));
      float hv[8], sm = 0.f, sq = 0.f;
#pragma unroll
      for (int i = 0; i < 8; ++i) { hv[i] = dec(v[i >> 2][i & 3]) - piv; sm += hv[i]; sq += hv[i] * hv[i]; }
      sm = wave_sum(sm) * (1.f / 512.f); sq = wave_sum(sq) * (1.f / 512.f);
      const float rs = __builtin_amdgcn_rsqf(fmaxf(sq - sm * sm, 0.f) + 1e-5f);
#pragma unroll
      for (int i = 0; i < 2; ++i)
        *(LAS u32x2*)(L + L_ACT + w * APITCH + i * 512 + lane * 8) = u32x2{cvtpk((hv[4 * i] - sm) * rs, (hv[4 * i + 1] - sm) * rs), cvtpk((hv[4 * i + 2] - sm) * rs, (hv[4 * i + 3] - sm) * rs)};
      BARRIER();
    }
    EXIT();
  }

  __builtin_amdgcn_s_setprio(2);
  unsigned long long eraw;
  asm volatile("s_load_dwordx2 %0, %1, 0x0" : "=s"(eraw) : "s"(ws) : "memory");
  const int hw = w - 4;
  unsigned long long enext;
  LAS unsigned char* LW = L + L_W + hw * L_WH;
  const int slice = split * 4 + b;
  {
    const rsrc_t rx = __builtin_amdgcn_make_buffer_rsrc((void*)x, 0, 4096, RSRC3);
    const rsrc_t rq = __builtin_amdgcn_make_buffer_rsrc((void*)wq, 0, 512 * 4096 * 2, RSRC3);
    const u32x4 xf = __builtin_amdgcn_raw_buffer_load_b128(rx, (n & 3) * 1024 + (hw * 128 + (n >> 2) * 32 + g * 8) * 2, 0, 0);
    u32x4 wr[4];
#pragma unroll
    for (int i = 0; i < 4; ++i)
      wr[i] = __builtin_amdgcn_raw_buffer_load_b128(rq, (hw * 128 + (lane >> 1)) * 8192 + (hkv * 512 + slice * 16) * 2 + (lane & 1) * 16, i * 32 * 8192, AUX_NT);
    __builtin_amdgcn_sched_barrier(0);
    asm volatile("s_waitcnt lgkmcnt(0)" : "+s"(eraw) :: "memory");
    enext = eraw + 0x9E3779B97F4A7C15ull;
    const unsigned tlo = (unsigned)enext & 0xffu, thi = (unsigned)(enext >> 8);
    if (EARLYB) BARRIER();
    if (hw == 2 && lane < 2) *(LAS unsigned*)(L + L_FLAG + lane * 4) = 0u;
    float sm = 0.f;
#pragma unroll
    for (int j = 0; j < 4; ++j) sm += bflo(xf[j]) + bfhi(xf[j]);
    const float mu = sum16(sm) * (1.f / 128.f);
    float sq = 0.f;
#pragma unroll
    for (int j = 0; j < 4; ++j) { const float d0 = bflo(xf[j]) - mu, d1 = bfhi(xf[j]) - mu; sq += d0 * d0; sq += d1 * d1; }
    sq = sum16(sq);
    LAS float* rq2 = (LAS float*)(L + L_RED2 + hw * 512);
    if (lane < 4) { rq2[80 + lane] = mu; rq2[84 + lane] = sq; }
#pragma unroll
    for (int i = 0; i < 4; ++i) *(LAS u32x4*)(LW + i * 1024 + lane * 16) = wr[i];
    LGKM0();
    f32x4 qa = {0.f, 0.f, 0.f, 0.f}, qo = {0.f, 0.f, 0.f, 0.f};
    const u32x4 ones = {0x3f803f80u, 0x3f803f80u, 0x3f803f80u, 0x3f803f80u};
#pragma unroll
    for (int i = 0; i < 4; ++i) {
      const bf16x8 a = trfrag(LW + (i * 32 + g * 8 + (n >> 2)) * 32 + (n & 3) * 8, 128);
      const bool mine = (n >> 2) == i;
      const u32x4 xm = {mine ? xf[0] : 0u, mine ? xf[1] : 0u, mine ? xf[2] : 0u, mine ? xf[3] : 0u};
      qa = mfma32(a, __builtin_bit_cast(bf16x8, xm), qa);
      qo = mfma32(a, __builtin_bit_cast(bf16x8, ones), qo);
    }
#pragma unroll
    for (int i = 0; i < 4; ++i) { qa[i] += dpp<0x124>(qa[i]); qa[i] += dpp<0x128>(qa[i]); }
    if (n < 5) *(LAS f32x4*)(rq2 + n * 16 + g * 4) = n < 4 ? qa : qo;
    LGKM0();
    if (lane == 0) *(volatile LAS unsigned long long*)(L + L_QF + hw * 8) = enext;
    if (hw == 0) {
#pragma nounroll
      for (unsigned spins = 0; spins < (SPIN_CAP << 4); ++spins) {
        const unsigned long long f = *(volatile LAS unsigned long long*)(L + L_QF + (lane & 3) * 8);
        if (__all(f == enext)) break;
      }
      asm volatile("" ::: "memory");
      const int bb = (lane >> 3) & 3, c2 = (lane & 7) * 2;
      f32x2 dx = {0.f, 0.f}, d1 = {0.f, 0.f};
      float mq[4], msum = 0.f, m2 = 0.f;
#pragma unroll
      for (int i = 0; i < 4; ++i) {
        const LAS float* p = (const LAS float*)(L + L_RED2 + i * 512);
        dx += *(const LAS f32x2*)(p + bb * 16 + c2); d1 += *(const LAS f32x2*)(p + 64 + c2);
        mq[i] = p[80 + bb]; msum += mq[i]; m2 += p[84 + bb];
      }
      const float mean = msum * 0.25f;
#pragma unroll
      for (int i = 0; i < 4; ++i) m2 += 128.f * (mq[i] - mean) * (mq[i] - mean);
      const float rs = (0.08838834764831845f * 1.4426950408889634f) * __builtin_amdgcn_rsqf(m2 * (1.f / 512.f) + 1e-5f);
      const float q0 = (dx[0] - mean * d1[0]) * rs, q1 = (dx[1] - mean * d1[1]) * rs;
      const u32x4 gq = {((asu(q0) + 0x80u) & 0xffffff00u) | tlo, thi, ((asu(q1) + 0x80u) & 0xffffff00u) | tlo, thi};
      __builtin_amdgcn_raw_buffer_store_b128(gq, rw, lane < 32 ? WS_Q + ((hkv * 4 + bb) * 512 + slice * 16 + c2) * 8 : OOB, 0, AUX_SC1);
    }
    STAMP(1);
    __builtin_amdgcn_s_sleep(QSLEEP);
    const int t = tid - 256;
    u32x4 v = {0u, 0u, 0u, 0u};
    unsigned bad = 1u;
#pragma nounroll
    for (unsigned spins = 0; spins < SPIN_CAP; ++spins) {
      if (bad) { v = ldpoll(rw, WS_Q + ((hkv * 4 + b) * 512 + 2 * t) * 8); bad = (v[1] ^ thi) | (v[3] ^ thi) | (((v[0] ^ tlo) | (v[2] ^ tlo)) & 0xffu); }
      if (__all(bad == 0u)) break;
      __builtin_amdgcn_s_sleep(QSLEEP2);
    }
    const float q0 = asf(v[0] & 0xffffff00u), q1 = asf(v[2] & 0xffffff00u);
    const unsigned hi = cvtpk(q0, q1), lo = cvtpk(q0 - bflo(hi), q1 - bfhi(hi));
    *(LAS unsigned*)(L + L_QI + t * 4) = hi;
    *(LAS unsigned*)(L + L_QI + 1024 + t * 4) = lo;
  }
  STAMP(2);
  BARRIER();
  if (role == 3) EXIT();
  const Tag tag = make_tag(enext);
  __builtin_amdgcn_s_setprio(0);
  const unsigned lw = (unsigned)(L_W + hw * L_WH) + (unsigned)(size_t)L;
#pragma nounroll
  for (unsigned spins = 0; spins < (SPIN_CAP << 2); ++spins) {
    if (role == 0 || !GWAIT ? ldsu(L + L_FLAG + 4) != 0u : ldsu(L + L_FLAG) == 4u) break;
    __builtin_amdgcn_s_sleep(4);
  }
  if (role == 0) {
    const int c = ridx, col0 = (ri >> 1) * 256 + hw * 64;
    const rsrc_t r0 = __builtin_amdgcn_make_buffer_rsrc((void*)(wo + (size_t)c * 64 * 512), 0, 65536, RSRC3);
    dma_run<0, 8, 0, 8192>(r0, lw, (lane >> 3) * 1024 + col0 * 2 + (lane & 7) * 16);
    STAMP(12);
    VM0();
    STAMP(3);
    bf16x8 wf[2][4];
#pragma unroll
    for (int ks = 0; ks < 2; ++ks)
#pragma unroll
      for (int t = 0; t < 4; ++t) wf[ks][t] = trfrag(LW + (ks * 32 + (n >> 2) * 8 + g) * 128 + (t * 16 + (n & 3) * 4) * 2, 512);
    int ao = L_ACT + (n & 3) * APITCH + g * 16, so = WS_B + (c * 2048 + (n & 3) * 512 + col0 + (n >> 2) * 16 + g * 4) * 4;
    PIN(ao); PIN(so);
    BARRIER();
    f32x4 acc[4];
#pragma unroll
    for (int ks = 0; ks < 2; ++ks) {
      const bf16x8 bf = *(LAS bf16x8*)(L + ao + ks * 64);
#pragma unroll
      for (int t = 0; t < 4; ++t) acc[t] = mfma32(wf[ks][t], bf, ks ? acc[t] : f32x4{0.f, 0.f, 0.f, 0.f});
    }
    st4(rw, so, pick4(acc[0], acc[1], acc[2], acc[3], n), tag);
    STAMP(6);
  } else if (role == 1) {
    const int k = ridx;
    {
      const rsrc_t r1 = __builtin_amdgcn_make_buffer_rsrc((void*)wg, 0, 512 * FF * 2, RSRC3);
      const rsrc_t r2 = __builtin_amdgcn_make_buffer_rsrc((void*)wu, 0, 512 * FF * 2, RSRC3);
      const rsrc_t r3 = __builtin_amdgcn_make_buffer_rsrc((void*)(wd + (size_t)k * 32 * 512), 0, 32768, RSRC3);
      const int vo = (hw * 128 + (lane >> 2)) * (FF * 2) + k * 64 + (lane & 3) * 16;
      dmag_run<0, 8, 0, 16 * FF * 2>(r1, lw, vo);
      dmag_run<0, 8, 8192, 16 * FF * 2>(r2, lw, vo);
      dma_run<0, 8, 16384, 4096>(r3, lw, g * 1024 + hw * 256 + n * 16);
    }
    asm volatile("s_waitcnt vmcnt(8)" ::: "memory");
    bf16x8 wf[4][4], df[8];
#pragma unroll
    for (int ks = 0; ks < 4; ++ks)
#pragma unroll
      for (int t = 0; t < 4; ++t) wf[ks][t] = trfrag(LW + (t >> 1) * 8192 + (ks * 32 + g * 8 + (n >> 2)) * 64 + ((t & 1) * 16 + (n & 3) * 4) * 2, 256);
    VM0();
    STAMP(3);
#pragma unroll
    for (int t = 0; t < 8; ++t) df[t] = trfrag(LW + 16384 + (g * 8 + (n >> 2)) * 256 + (t * 16 + (n & 3) * 4) * 2, 1024);
    int ao = L_ACT + (n & 3) * APITCH + (hw * 128 + g * 8) * 2, ro = L_RED2 + hw * 1024 + (n & 3) * 256 + ((n >> 2) * 16 + g * 4) * 4, ri2 = L_RED2 + g * 256 + n * 8;
    int wo2 = L_AI + hw * 256 + g * 64 + n * 4, bo = L_AI + hw * 256 + (n & 3) * 64 + g * 16, so = WS_D + (k * 2048 + (n & 3) * 512 + hw * 128 + (n >> 2) * 16 + g * 4) * 4;
    PIN(ao); PIN(ro); PIN(ri2); PIN(wo2); PIN(bo); PIN(so);
    BARRIER();
    STAMP(7);
    f32x4 acc[8];
    {
      bf16x8 bf[4];
#pragma unroll
      for (int ks = 0; ks < 4; ++ks) bf[ks] = *(LAS bf16x8*)(L + ao + ks * 64);
#pragma unroll
      for (int ks = 0; ks < 4; ++ks)
#pragma unroll
        for (int t = 0; t < 4; ++t) acc[t] = mfma32(wf[ks][t], bf[ks], ks ? acc[t] : f32x4{0.f, 0.f, 0.f, 0.f});
    }
    *(LAS f32x4*)(L + ro) = pick4(acc[0], acc[1], acc[2], acc[3], n);
    BARRIER();
    {
      f32x2 gp[4], up[4];
#pragma unroll
      for (int i = 0; i < 4; ++i) { gp[i] = *(const LAS f32x2*)(L + ri2 + i * 1024); up[i] = *(const LAS f32x2*)(L + ri2 + i * 1024 + 128); }
      const f32x2 gs = (gp[0] + gp[1]) + (gp[2] + gp[3]), us = (up[0] + up[1]) + (up[2] + up[3]);
      float a[2];
#pragma unroll
      for (int i = 0; i < 2; ++i) {
        const float z = gs[i] * (-2.302208198f - 0.1029432397f * gs[i] * gs[i]);
        const float sg = __builtin_amdgcn_rcpf(1.f + __builtin_amdgcn_exp2f(z));
        a[i] = (gs[i] * us[i]) * sg;
      }
      *(LAS unsigned*)(L + wo2) = cvtpk(a[0], a[1]);
    }
    LGKM0();
    {
      const bf16x8 bf = *(LAS bf16x8*)(L + bo);
#pragma unroll
      for (int t = 0; t < 8; ++t) acc[t] = mfma32(df[t], bf, f32x4{0.f, 0.f, 0.f, 0.f});
#pragma unroll
      for (int hf = 0; hf < 2; ++hf) st4(rw, so + hf * 256, pick4(acc[4 * hf], acc[4 * hf + 1], acc[4 * hf + 2], acc[4 * hf + 3], n), tag);
    }
    STAMP(6);
  } else {
    const int u = ridx * 4 + hw;
    const int p = lane & 3, cs = lane >> 2;
    const int pos = u * 16 + p * 4 + (g & 1) * 2 + (g >> 1);
    const float xv = bflo(*(const GAS unsigned short*)(x + pos));
    const unsigned tg = (g & 1) ? ((g & 2) ? tag.t[3] : tag.t[2]) : ((g & 2) ? tag.t[1] : tag.t[0]);
    float h;
    {
      int off[4];
#pragma unroll
      for (int i = 0; i < 4; ++i) off[i] = WS_B + ((i * 16 + cs) * 2048 + u * 16 + p * 4) * 4;
      u32x4 v[4];
      gather<4, TSENT>(rw, off, v, tag, lane);
      STAMP(5);
      const f32x4 sv = (dec4(v[0]) + dec4(v[1])) + (dec4(v[2]) + dec4(v[3]));
      h = tr32(tr16(sv[0], sv[2]), tr16(sv[1], sv[3]));
      h += dpp<0x124>(h); h += dpp<0x128>(h);
      h += xv;
      __builtin_amdgcn_raw_buffer_store_b32(enc(h, tg), rw, (cs & 3) < CREP ? WS_C + ((cs & 3) * 2048 + pos) * 4 : OOB, 0, AUX_SC1);
      STAMP(6);
    }
    {
      int off[4];
#pragma unroll
      for (int i = 0; i < 4; ++i) off[i] = WS_D + ((i * 16 + (i == 3 ? (cs & 7) : cs)) * 2048 + u * 16 + p * 4) * 4;
      u32x4 v[4];
      gather<4, TSENT>(rw, off, v, tag, lane);
      STAMP(7);
      if (cs >= 8) v[3] = u32x4{0u, 0u, 0u, 0u};
      const f32x4 sv = (dec4(v[0]) + dec4(v[1])) + (dec4(v[2]) + dec4(v[3]));
      float r = tr32(tr16(sv[0], sv[2]), tr16(sv[1], sv[3]));
      r += dpp<0x124>(r); r += dpp<0x128>(r);
      r += h;
      if ((cs & 3) == 0) *(GAS unsigned short*)(out + pos) = (unsigned short)cvtpk(r, r);
      if (u == 0) __builtin_amdgcn_raw_buffer_store_b64(u32x2{(unsigned)enext, (unsigned)(enext >> 32)}, rw, lane == 0 ? WS_E : OOB, 0, AUX_SC1);
      STAMP(8);
    }
  }
  EXIT();
}

extern "C" void kernel_launch(void* const* d_in, const int* in_sizes, int n_in, void* d_out, int out_size, void* d_ws, size_t ws_size,
                              hipStream_t stream) {
  (void)hipFuncSetAttribute((const void*)fused_decode_block, hipFuncAttributeMaxDynamicSharedMemorySize, L_END);
  fused_decode_block<<<256, 512, L_END, stream>>>((const unsigned short*)d_in[0], (const unsigned short*)d_in[1], (const unsigned short*)d_in[2],
                                                  (const unsigned short*)d_in[3], (const unsigned short*)d_in[4], (const unsigned short*)d_in[6],
                                                  (const unsigned short*)d_in[7], (const unsigned short*)d_in[8], (unsigned short*)d_out, (unsigned char*)d_ws);
}
```

```cpp
#include <hip/hip_runtime.h>
#include <hip/hip_bf16.h>
#include <cstdint>

typedef unsigned u32x4 __attribute__((ext_vector_type(4)));
typedef unsigned u32x2 __attribute__((ext_vector_type(2)));
typedef float f32x4 __attribute__((ext_vector_type(4)));
typedef float f32x2 __attribute__((ext_vector_type(2)));
typedef short bf16x8 __attribute__((ext_vector_type(8)));
typedef short bf16x4 __attribute__((ext_vector_type(4)));
#define LAS __attribute__((address_space(3)))
#define GAS __attribute__((address_space(1)))
typedef __amdgpu_buffer_rsrc_t rsrc_t;

constexpr int SK = 16384, DH = 128, FF = 1792;
constexpr int NPK = 12;
constexpr int PACE_T = 5650;
constexpr int PACE_C = -50;
constexpr int PACE_CAP = 150;
constexpr int AUX_NT = 2, AUX_SC1 = 16, RSRC3 = 0x00020000;
constexpr unsigned SPIN_CAP = 1u << 16;
constexpr int OOB = 0x7ffffff0;

constexpr int WS_E = 0;
constexpr int WS_Q = 4096;
constexpr int WS_A = WS_Q + 16384 * 8;
constexpr int WS_ML = WS_A + 131072 * 4;
constexpr int WS_B = WS_ML + 1024 * 16;
constexpr int WS_C = WS_B + 131072 * 4;
constexpr int WS_D = WS_C + 4 * 2048 * 4;
constexpr int WS_END = WS_D + 56 * 2048 * 4;

constexpr int L_W = 0;
constexpr int L_WH = 24576;
constexpr int L_ACT = 4 * L_WH;
constexpr int APITCH = 1040;
constexpr int L_RED = L_ACT + 4 * APITCH;
constexpr int L_ML = L_RED + 8192;
constexpr int L_AI = L_ML + 128;
constexpr int L_QI = L_AI + 1024;
constexpr int L_RED2 = L_QI + 2048;
constexpr int L_QF = L_RED2 + 4096;
constexpr int L_FLAG = L_QF + 32;
constexpr int L_T1 = L_FLAG + 16;
constexpr int L_END = L_T1 + 16;

__device__ __forceinline__ unsigned cvtpk(float lo, float hi) {
  typedef __bf16 bf2_t __attribute__((ext_vector_type(2)));
  f32x2 v = {lo, hi}; return __builtin_bit_cast(unsigned, __builtin_convertvector(v, bf2_t));
}
__device__ __forceinline__ float bflo(unsigned u) { return __builtin_bit_cast(float, u << 16); }
__device__ __forceinline__ float bfhi(unsigned u) { return __builtin_bit_cast(float, u & 0xffff0000u); }
__device__ __forceinline__ float asf(unsigned u) { return __builtin_bit_cast(float, u); }
__device__ __forceinline__ unsigned asu(float f) { return __builtin_bit_cast(unsigned, f); }
template <int CTRL> __device__ __forceinline__ float dpp(float x) {
  return __builtin_bit_cast(float, __builtin_amdgcn_mov_dpp(__builtin_bit_cast(int, x), CTRL, 0xf, 0xf, true));
}
__device__ __forceinline__ float xg_max(float x) {
  auto s = __builtin_amdgcn_permlane16_swap(asu(x), asu(x), false, false);
  x = fmaxf(asf(s[0]), asf(s[1]));
  auto t = __builtin_amdgcn_permlane32_swap(asu(x), asu(x), false, false);
  return fmaxf(asf(t[0]), asf(t[1]));
}
__device__ __forceinline__ float tr16(float a, float b) { auto s = __builtin_amdgcn_permlane16_swap(asu(a), asu(b), false, false); return asf(s[0]) + asf(s[1]); }
__device__ __forceinline__ float tr32(float a, float b) { auto s = __builtin_amdgcn_permlane32_swap(asu(a), asu(b), false, false); return asf(s[0]) + asf(s[1]); }
__device__ __forceinline__ float xg_sum(float x) { x = tr16(x, x); return tr32(x, x); }
__device__ __forceinline__ float sum16(float x) { x += dpp<0x124>(x); x += dpp<0x128>(x); return xg_sum(x); }
__device__ __forceinline__ float wave_sum(float x) { x += dpp<0xB1>(x); x += dpp<0x4E>(x); return sum16(x); }
#define BARRIER() do { __builtin_amdgcn_sched_barrier(0); asm volatile("s_waitcnt lgkmcnt(0)\n\ts_barrier" ::: "memory"); __builtin_amdgcn_sched_barrier(0); } while (0)
#define LGKM0() asm volatile("s_waitcnt lgkmcnt(0)" ::: "memory")
#define VM0() asm volatile("s_waitcnt vmcnt(0)" ::: "memory")
#define PIN(x) asm volatile("" : "+v"(x))

__device__ __forceinline__ u32x4 ldpoll(rsrc_t rs, int off) {
  asm volatile("" : "+v"(off) :: "memory");
  return __builtin_amdgcn_raw_buffer_load_b128(rs, off, 0, AUX_SC1);
}
__device__ __forceinline__ unsigned ldsu(LAS unsigned char* p) { return __builtin_amdgcn_readfirstlane(*(volatile LAS unsigned*)p); }
struct Tag { unsigned t[4]; };
__device__ __forceinline__ unsigned enc(float v, unsigned t) { return ((asu(v) + 0x800u) & 0xfffff000u) | t; }
__device__ __forceinline__ float dec(unsigned u) { return asf(u & 0xfffff000u); }
__device__ __forceinline__ f32x4 dec4(u32x4 v) { return f32x4{dec(v[0]), dec(v[1]), dec(v[2]), dec(v[3])}; }
__device__ __forceinline__ void st4(rsrc_t rs, int off, f32x4 v, const Tag& g) {
  __builtin_amdgcn_raw_buffer_store_b128(u32x4{enc(v[0], g.t[0]), enc(v[1], g.t[1]), enc(v[2], g.t[2]), enc(v[3], g.t[3])}, rs, off, 0, AUX_SC1);
}
__device__ __forceinline__ unsigned bad4(u32x4 v, const Tag& g) { return (v[0] ^ g.t[0]) | (v[1] ^ g.t[1]) | (v[2] ^ g.t[2]) | (v[3] ^ g.t[3]); }
__device__ __forceinline__ Tag make_tag(unsigned long long e) {
  Tag g{{(unsigned)e & 0xfffu, (unsigned)(e >> 12) & 0xfffu, (unsigned)(e >> 24) & 0xfffu, (unsigned)(e >> 36) & 0xfffu}};
  asm volatile("" : "+v"(g.t[0]), "+v"(g.t[1]), "+v"(g.t[2]), "+v"(g.t[3]));
  return g;
}
template <int N, bool SENT> __device__ __forceinline__ void gather(rsrc_t rs, const int (&off)[N], u32x4 (&v)[N], const Tag& g) {
  if (SENT) {
#pragma nounroll
    for (unsigned spins = 0; spins < SPIN_CAP; ++spins) {
      const u32x4 s0 = ldpoll(rs, off[0]);
      if (__any((bad4(s0, g) & 0xfffu) == 0u)) break;
      __builtin_amdgcn_s_sleep(2);
    }
  }
  unsigned bad = 1u;
#pragma unroll
  for (int i = 0; i < N; ++i) v[i] = u32x4{0u, 0u, 0u, 0u};
#pragma nounroll
  for (unsigned spins = 0; spins < SPIN_CAP; ++spins) {
    if (bad) {
#pragma unroll
      for (int i = 0; i < N; ++i) v[i] = ldpoll(rs, off[i]);
      bad = 0u;
#pragma unroll
      for (int i = 0; i < N; ++i) bad |= bad4(v[i], g);
      bad &= 0xfffu;
    }
    if (__all(bad == 0u)) break;
  }
  asm volatile("" ::: "memory");
}
#define DMA16(NAME, POL) \
template <int LOFF, int SOFF> __device__ __forceinline__ void NAME(rsrc_t rs, unsigned lds, int voff) { \
  unsigned keep, so; \
  asm volatile("s_mov_b32 %0, m0\n\ts_add_u32 m0, %3, %5\n\ts_mov_b32 %1, %6\n\tbuffer_load_dwordx4 %2, %4, %1 offen " POL "lds\n\ts_mov_b32 m0, %0" \
               : "=&s"(keep), "=&s"(so) : "v"(voff), "s"(lds), "s"(rs), "i"(LOFF), "i"(SOFF) : "memory", "scc"); \
} \
template <int I, int N, int L0, int SSTR> __device__ __forceinline__ void NAME##_run(rsrc_t rs, unsigned lds, int voff) { \
  if constexpr (I < N) { NAME<L0 + I * 1024, I * SSTR>(rs, lds, voff); NAME##_run<I + 1, N, L0, SSTR>(rs, lds, voff); } \
}
DMA16(dma, "nt ")
DMA16(dmac, "")
__device__ __forceinline__ bf16x8 trfrag(LAS unsigned char* p, int second) {
  typedef short s4 __attribute__((ext_vector_type(4)));
  const s4 a = __builtin_amdgcn_ds_read_tr16_b64_v4i16((LAS s4*)p);
  const s4 b = __builtin_amdgcn_ds_read_tr16_b64_v4i16((LAS s4*)(p + second));
  return __builtin_shufflevector(a, b, 0, 1, 2, 3, 4, 5, 6, 7);
}
__device__ __forceinline__ f32x4 mfma32(bf16x8 a, bf16x8 b, f32x4 c) { return __builtin_amdgcn_mfma_f32_16x16x32_bf16(a, b, c, 0, 0, 0); }
__device__ __forceinline__ f32x4 pick4(f32x4 a0, f32x4 a1, f32x4 a2, f32x4 a3, int n) {
  f32x4 r;
#pragma unroll
  for (int i = 0; i < 4; ++i) r[i] = (n & 8) ? ((n & 4) ? a3[i] : a2[i]) : ((n & 4) ? a1[i] : a0[i]);
  return r;
}

__global__ void __launch_bounds__(512, 2) fused_decode_block(const unsigned short* __restrict__ kc, const unsigned short* __restrict__ vc, const unsigned short* __restrict__ x,
                                                             const unsigned short* __restrict__ wq, unsigned char* ws, unsigned short* out, const unsigned short* __restrict__ wo,
                                                             const unsigned short* __restrict__ wg, const unsigned short* __restrict__ wu, const unsigned short* __restrict__ wd) {
  extern __shared__ __attribute__((aligned(16))) unsigned char smem[];
  LAS unsigned char* L = (LAS unsigned char*)smem;
  const int tid = threadIdx.x, lane = tid & 63, w = __builtin_amdgcn_readfirstlane(tid >> 6), blk = blockIdx.x;
  const int g = lane >> 4, n = lane & 15;
  const int pair = blk >> 3, split = blk & 7;
  const int b = pair >> 3, hkv = pair & 7;
#define ROLE() \
  int pr_ = pair; asm volatile("" : "+s"(pr_)); \
  const int ri = (split + pr_) & 7; \
  const int ridx = ri < 4 ? pr_ * 2 + (ri & 1) : ri == 4 ? pr_ * 2 : ri == 5 ? ((pr_ + 31) & 31) * 2 + 1 : pr_; \
  const int role = ri < 4 ? 0 : ri < 6 ? (ridx < 56 ? 1 : 3) : ri == 6 ? 2 : 3;

  if (__builtin_expect(w < 4, 1)) {
    const rsrc_t rk = __builtin_amdgcn_make_buffer_rsrc((void*)(kc + (size_t)pair * SK * DH), 0, SK * 256, RSRC3);
    const rsrc_t rv = __builtin_amdgcn_make_buffer_rsrc((void*)(vc + (size_t)pair * SK * DH), 0, SK * 256, RSRC3);
    const int koff = (4 * (n & 3) + ((n >> 2) & 1)) * 256 + (n >> 3) * 64 + 16 * g, voff = lane * 16;
    const int t0 = split * 128 + w;
#define TOFF(t) ((t0 + (t) * 4) * 4096)
#define LOADG(q, i) ((q) < 32 ? __builtin_amdgcn_raw_buffer_load_b128(rk, koff + ((i) >> 1) * 512 + ((i) & 1) * 128, TOFF((q) & 31), AUX_NT) \
                              : __builtin_amdgcn_raw_buffer_load_b128(rv, voff + (i) * 1024, TOFF((q) & 31), AUX_NT))
#define SLOT(q) ((q) < NPK ? (q) : ((q) - NPK) & 3)
#define NEXT(q) ((q) < 4 ? (q) + NPK : (q) < NPK ? 64 : (q) + 4)
    unsigned t1 = 0u;
    const unsigned tst = (unsigned)__builtin_amdgcn_s_memrealtime();
#define PACE(q) asm volatile("1:\n\ts_memrealtime s[98:99]\n\ts_waitcnt lgkmcnt(0)\n\ts_sub_u32 s98, s98, %0\n\ts_cmp_lt_i32 s98, %1\n\ts_cbranch_scc0 2f\n\ts_sleep 1\n\ts_branch 1b\n2:" \
                             :: "s"(t1), "i"(PACE_C + ((q) * PACE_T) / 100) : "scc", "memory", "s98", "s99")
    u32x4 R[NPK][4];
#pragma unroll
    for (int q = 0; q < NPK; ++q) {
      if (q == 2) { __builtin_amdgcn_s_waitcnt(0x0F77); t1 = (unsigned)__builtin_amdgcn_s_memrealtime(); __builtin_amdgcn_sched_barrier(0); }
#pragma unroll
      for (int i = 0; i < 4; ++i) R[q][i] = LOADG(q, i);
      __builtin_amdgcn_sched_barrier(0);
    }
    if (lane == 0) *(LAS unsigned*)(L + L_T1 + w * 4) = t1;
    BARRIER();
    {
      const u32x4 tt = *(LAS u32x4*)(L + L_T1);
      const int d = min(min((int)(tt[0] - t1), (int)(tt[1] - t1)), min((int)(tt[2] - t1), (int)(tt[3] - t1)));
      t1 = __builtin_amdgcn_readfirstlane(t1 + (unsigned)d);
      if ((int)(t1 - (tst + PACE_CAP)) > 0) t1 = tst + PACE_CAP;
    }
    bf16x8 qf[2];
#pragma unroll
    for (int j = 0; j < 2; ++j) qf[j] = *(LAS bf16x8*)(L + L_QI + (n & 1) * 1024 + (n >> 2) * 256 + j * 128 + ((n >> 1) & 1) * 64 + g * 16);
    f32x4 S[32];
    float m = -INFINITY;
#pragma unroll
    for (int q = 0; q < 32; ++q) {
      f32x4 D[2];
      if (NEXT(q) < 64) PACE(NEXT(q));
#pragma unroll
      for (int i = 0; i < 4; ++i) {
        D[i >> 1] = mfma32(__builtin_bit_cast(bf16x8, R[SLOT(q)][i]), qf[i & 1], (i & 1) ? D[i >> 1] : f32x4{0.f, 0.f, 0.f, 0.f});
        __builtin_amdgcn_sched_barrier(0);
        if (NEXT(q) < 64) R[SLOT(q)][i] = LOADG(NEXT(q), i);
        __builtin_amdgcn_sched_barrier(0);
      }
#pragma unroll
      for (int i = 0; i < 4; ++i) {
        auto sw = __builtin_amdgcn_permlane32_swap(asu(D[0][i]), asu(D[1][i]), false, false);
        float z = (n & 2) ? asf(sw[1]) : asf(sw[0]);
        z += dpp<0xB1>(z); z += dpp<0x4E>(z);
        S[q][i] = z;
      }
      m = fmaxf(fmaxf(m, fmaxf(S[q][0], S[q][1])), fmaxf(S[q][2], S[q][3]));
      __builtin_amdgcn_sched_barrier(0);
    }
    m = xg_max(m);
    const Tag tag = make_tag(((unsigned long long)ldsu(L + L_QF + 4) << 32) | ldsu(L + L_QF));
    ROLE();
    const rsrc_t rw = __builtin_amdgcn_make_buffer_rsrc((void*)ws, 0, WS_END, RSRC3);
    const unsigned M0 = (n & 2) ? 0u : ((n & 1) ? 0xffff0000u : 0xffffu), M1 = (n & 2) ? ((n & 1) ? 0xffff0000u : 0xffffu) : 0u;
    f32x4 olo = {0.f, 0.f, 0.f, 0.f}, ohi = {0.f, 0.f, 0.f, 0.f};
    float l = 0.f;
#pragma unroll
    for (int q = 32; q < 64; ++q) {
      if (q == 60 && tid == 0) *(volatile LAS unsigned*)(L + L_FLAG + 4) = 1u;
      if (NEXT(q) < 64) PACE(NEXT(q));
#pragma unroll
      for (int i = 0; i < 4; ++i) {
        const float p = __builtin_amdgcn_exp2f(S[q - 32][i] - m);
        const unsigned pk = cvtpk(p, p);
        l += bfhi(pk);
        const u32x2 bb = {pk & M0, pk & M1};
        const u32x4 vv = R[SLOT(q)][i];
        const u32x2 alo = {vv[0], vv[1]}, ahi = {vv[2], vv[3]};
        olo = __builtin_amdgcn_mfma_f32_16x16x16bf16_1k(__builtin_bit_cast(bf16x4, alo), __builtin_bit_cast(bf16x4, bb), olo, 0, 0, 0);
        ohi = __builtin_amdgcn_mfma_f32_16x16x16bf16_1k(__builtin_bit_cast(bf16x4, ahi), __builtin_bit_cast(bf16x4, bb), ohi, 0, 0, 0);
        __builtin_amdgcn_sched_barrier(0);
        if (NEXT(q) < 64) R[SLOT(q)][i] = LOADG(NEXT(q), i);
        __builtin_amdgcn_sched_barrier(0);
      }
    }
    *(LAS f32x4*)(L + L_RED + (w * 64 + lane) * 32) = olo;
    *(LAS f32x4*)(L + L_RED + (w * 64 + lane) * 32 + 16) = ohi;
    l = xg_sum(l);
    if (lane < 16 && (n & 3) == 0) *(LAS f32x2*)(L + L_ML + ((n >> 2) * 4 + w) * 8) = f32x2{m, l};
    LGKM0();
    if (lane == 0) __hip_atomic_fetch_add((LAS unsigned*)(L + L_FLAG), 1u, __ATOMIC_RELAXED, __HIP_MEMORY_SCOPE_WORKGROUP);
    const int hq = hkv * 4 + w, j = lane & 31;
    int poff = lane < 32 ? WS_A + ((((hq * 2 + (j >> 4)) * 4 + b) * 8 + split) * 64 + (j & 15) * 4) * 4 : lane == 32 ? WS_ML + ((hq * 4 + b) * 8 + split) * 16 : OOB;
    int src = L_RED + ((j >> 3) * 16 + w * 4 + ((j >> 1) & 3)) * 32 + (j & 1) * 16;
    PIN(poff); PIN(src);
#pragma nounroll
    for (unsigned spins = 0; spins < (SPIN_CAP << 4); ++spins) { if (ldsu(L + L_FLAG) == 4u) break; }
    asm volatile("" ::: "memory");
    {
      const f32x4 ml0 = *(const LAS f32x4*)(L + L_ML + w * 32), ml1 = *(const LAS f32x4*)(L + L_ML + w * 32 + 16);
      f32x4 p[4];
#pragma unroll
      for (int i = 0; i < 4; ++i) p[i] = *(const LAS f32x4*)(L + src + i * 2048);
      const float M = fmaxf(fmaxf(ml0[0], ml0[2]), fmaxf(ml1[0], ml1[2]));
      const float s0 = __builtin_amdgcn_exp2f(ml0[0] - M), s1 = __builtin_amdgcn_exp2f(ml0[2] - M), s2 = __builtin_amdgcn_exp2f(ml1[0] - M), s3 = __builtin_amdgcn_exp2f(ml1[2] - M);
      f32x4 o = (s0 * p[0] + s1 * p[1]) + (s2 * p[2] + s3 * p[3]);
      const float Ls = (s0 * ml0[1] + s1 * ml0[3]) + (s2 * ml1[1] + s3 * ml1[3]);
      const float Mh = dec(enc(M, 0u));
      if (lane >= 32) o = f32x4{Mh, Ls, M - Mh, Ls};
      st4(rw, poff, o, tag);
    }
    if (role == 0) {
      const int c = ridx, sp = lane >> 3, p0 = (lane & 7) * 8;
      const int o0 = WS_A + (((c * 4 + w) * 8 + sp) * 64 + p0) * 4;
      const int off[3] = {o0, o0 + 16, WS_ML + (((c >> 1) * 4 + w) * 8 + sp) * 16};
      int ao = L_ACT + w * APITCH + (p0 + 4 * (g & 1) + 2 * (g >> 1)) * 2;
      PIN(ao);
      u32x4 v[3];
      gather<3, false>(rw, off, v, tag);
      const float Ms = dec(v[2][0]) + dec(v[2][2]);
      float M = fmaxf(Ms, dpp<0x128>(Ms)); M = xg_max(M);
      const float sc = __builtin_amdgcn_exp2f(Ms - M);
      float Ls = dec(v[2][1]) * sc; Ls += dpp<0x128>(Ls); Ls = xg_sum(Ls);
      const float f = sc * __builtin_amdgcn_rcpf(Ls);
      float a[4];
#pragma unroll
      for (int i = 0; i < 4; ++i) a[i] = tr16(dec(v[0][i]) * f, dec(v[1][i]) * f);
      float e0 = tr32(a[0], a[2]), e1 = tr32(a[1], a[3]);
      e0 += dpp<0x128>(e0); e1 += dpp<0x128>(e1);
      *(LAS unsigned*)(L + ao) = cvtpk(e0, e1);
      BARRIER();
    } else if (role == 1) {
      const int o0 = WS_C + ((ridx & 3) * 2048 + w * 512 + lane * 4) * 4;
      const int off[2] = {o0, o0 + 1024};
      u32x4 v[2];
      gather<2, true>(rw, off, v, tag);
      const float piv = dec(__builtin_amdgcn_readfirstlane(v[0][0]));
      float hv[8], sm = 0.f, sq = 0.f;
#pragma unroll
      for (int i = 0; i < 8; ++i) { hv[i] = dec(v[i >> 2][i & 3]) - piv; sm += hv[i]; sq += hv[i] * hv[i]; }
      sm = wave_sum(sm) * (1.f / 512.f); sq = wave_sum(sq) * (1.f / 512.f);
      const float rs = __builtin_amdgcn_rsqf(fmaxf(sq - sm * sm, 0.f) + 1e-5f);
#pragma unroll
      for (int i = 0; i < 2; ++i)
        *(LAS u32x2*)(L + L_ACT + w * APITCH + i * 512 + lane * 8) = u32x2{cvtpk((hv[4 * i] - sm) * rs, (hv[4 * i + 1] - sm) * rs), cvtpk((hv[4 * i + 2] - sm) * rs, (hv[4 * i + 3] - sm) * rs)};
      BARRIER();
    }
    return;
  }

  __builtin_amdgcn_s_setprio(2);
  unsigned long long eraw;
  asm volatile("s_load_dwordx2 %0, %1, 0x0" : "=s"(eraw) : "s"(ws) : "memory");
  const int hw = w - 4;
  const rsrc_t rw = __builtin_amdgcn_make_buffer_rsrc((void*)ws, 0, WS_END, RSRC3);
  unsigned long long enext;
  LAS unsigned char* LW = L + L_W + hw * L_WH;
  const int slice = ((split + hkv) & 7) * 4 + b;
  {
    const rsrc_t rx = __builtin_amdgcn_make_buffer_rsrc((void*)x, 0, 4096, RSRC3);
    const rsrc_t rq = __builtin_amdgcn_make_buffer_rsrc((void*)wq, 0, 512 * 4096 * 2, RSRC3);
    const u32x4 xf = __builtin_amdgcn_raw_buffer_load_b128(rx, (n & 3) * 1024 + (hw * 128 + (n >> 2) * 32 + g * 8) * 2, 0, 0);
    u32x4 wr[4];
#pragma unroll
    for (int i = 0; i < 4; ++i)
      wr[i] = __builtin_amdgcn_raw_buffer_load_b128(rq, (hw * 128 + (lane >> 1)) * 8192 + (hkv * 512 + slice * 16) * 2 + (lane & 1) * 16, i * 32 * 8192, AUX_NT);
    __builtin_amdgcn_sched_barrier(0);
    if (hw == 2 && lane < 2) *(LAS unsigned*)(L + L_FLAG + lane * 4) = 0u;
    float sm = 0.f;
#pragma unroll
    for (int j = 0; j < 4; ++j) sm += bflo(xf[j]) + bfhi(xf[j]);
    const float mu = sum16(sm) * (1.f / 128.f);
    float sq = 0.f;
#pragma unroll
    for (int j = 0; j < 4; ++j) { const float d0 = bflo(xf[j]) - mu, d1 = bfhi(xf[j]) - mu; sq += d0 * d0; sq += d1 * d1; }
    sq = sum16(sq);
    LAS float* rq2 = (LAS float*)(L + L_RED2 + hw * 512);
    if (lane < 4) { rq2[80 + lane] = mu; rq2[84 + lane] = sq; }
#pragma unroll
    for (int i = 0; i < 4; ++i) *(LAS u32x4*)(LW + i * 1024 + lane * 16) = wr[i];
    asm volatile("s_waitcnt lgkmcnt(0)" : "+s"(eraw) :: "memory");
    enext = eraw + 0x9E3779B97F4A7C15ull;
    const unsigned tlo = (unsigned)enext & 0xffu, thi = (unsigned)(enext >> 8);
    f32x4 qa = {0.f, 0.f, 0.f, 0.f}, qo = {0.f, 0.f, 0.f, 0.f};
    const u32x4 ones = {0x3f803f80u, 0x3f803f80u, 0x3f803f80u, 0x3f803f80u};
#pragma unroll
    for (int i = 0; i < 4; ++i) {
      const bf16x8 a = trfrag(LW + (i * 32 + g * 8 + (n >> 2)) * 32 + (n & 3) * 8, 128);
      const bool mine = (n >> 2) == i;
      const u32x4 xm = {mine ? xf[0] : 0u, mine ? xf[1] : 0u, mine ? xf[2] : 0u, mine ? xf[3] : 0u};
      qa = mfma32(a, __builtin_bit_cast(bf16x8, xm), qa);
      qo = mfma32(a, __builtin_bit_cast(bf16x8, ones), qo);
    }
#pragma unroll
    for (int i = 0; i < 4; ++i) { qa[i] += dpp<0x124>(qa[i]); qa[i] += dpp<0x128>(qa[i]); }
    if (n < 5) *(LAS f32x4*)(rq2 + n * 16 + g * 4) = n < 4 ? qa : qo;
    LGKM0();
    if (lane == 0) *(volatile LAS unsigned long long*)(L + L_QF + hw * 8) = enext;
    if (hw == 0) {
#pragma nounroll
      for (unsigned spins = 0; spins < (SPIN_CAP << 4); ++spins) {
        const unsigned long long f = *(volatile LAS unsigned long long*)(L + L_QF + (lane & 3) * 8);
        if (__all(f == enext)) break;
      }
      asm volatile("" ::: "memory");
      const int bb = (lane >> 3) & 3, c2 = (lane & 7) * 2;
      f32x2 dx = {0.f, 0.f}, d1 = {0.f, 0.f};
      float mq[4], msum = 0.f, m2 = 0.f;
#pragma unroll
      for (int i = 0; i < 4; ++i) {
        const LAS float* p = (const LAS float*)(L + L_RED2 + i * 512);
        dx += *(const LAS f32x2*)(p + bb * 16 + c2); d1 += *(const LAS f32x2*)(p + 64 + c2);
        mq[i] = p[80 + bb]; msum += mq[i]; m2 += p[84 + bb];
      }
      const float mean = msum * 0.25f;
#pragma unroll
      for (int i = 0; i < 4; ++i) m2 += 128.f * (mq[i] - mean) * (mq[i] - mean);
      const float rs = (0.08838834764831845f * 1.4426950408889634f) * __builtin_amdgcn_rsqf(m2 * (1.f / 512.f) + 1e-5f);
      const float q0 = (dx[0] - mean * d1[0]) * rs, q1 = (dx[1] - mean * d1[1]) * rs;
      const u32x4 gq = {((asu(q0) + 0x80u) & 0xffffff00u) | tlo, thi, ((asu(q1) + 0x80u) & 0xffffff00u) | tlo, thi};
      __builtin_amdgcn_raw_buffer_store_b128(gq, rw, lane < 32 ? WS_Q + ((hkv * 4 + bb) * 512 + slice * 16 + c2) * 8 : OOB, 0, AUX_SC1);
    }
    __builtin_amdgcn_s_sleep(40);
    const int t = tid - 256;
    u32x4 v = {0u, 0u, 0u, 0u};
    unsigned bad = 1u;
#pragma nounroll
    for (unsigned spins = 0; spins < SPIN_CAP; ++spins) {
      if (bad) { v = ldpoll(rw, WS_Q + ((hkv * 4 + b) * 512 + 2 * t) * 8); bad = (v[1] ^ thi) | (v[3] ^ thi) | (((v[0] ^ tlo) | (v[2] ^ tlo)) & 0xffu); }
      if (__all(bad == 0u)) break;
      __builtin_amdgcn_s_sleep(10);
    }
    const float q0 = asf(v[0] & 0xffffff00u), q1 = asf(v[2] & 0xffffff00u);
    const unsigned hi = cvtpk(q0, q1), lo = cvtpk(q0 - bflo(hi), q1 - bfhi(hi));
    *(LAS unsigned*)(L + L_QI + t * 4) = hi;
    *(LAS unsigned*)(L + L_QI + 1024 + t * 4) = lo;
  }
  BARRIER();
  if (tid == 511) {
    const unsigned t = __builtin_nontemporal_load((const GAS unsigned*)(vc + ((size_t)pair * SK + split * 2048) * DH));
    asm volatile("" :: "v"(t));
  }
  ROLE();
  if (role == 3) return;
  const Tag tag = make_tag(enext);
  __builtin_amdgcn_s_setprio(0);
  const unsigned lw = (unsigned)(L_W + hw * L_WH) + (unsigned)(size_t)L;
#pragma nounroll
  for (unsigned spins = 0; spins < (SPIN_CAP << 2); ++spins) {
    if (role == 0 ? ldsu(L + L_FLAG + 4) != 0u : ldsu(L + L_FLAG) == 4u) break;
    __builtin_amdgcn_s_sleep(4);
  }
  if (role == 0) {
    const int c = ridx, col0 = (ri >> 1) * 256 + hw * 64;
    const rsrc_t r0 = __builtin_amdgcn_make_buffer_rsrc((void*)(wo + (size_t)c * 64 * 512), 0, 65536, RSRC3);
    dma_run<0, 8, 0, 8192>(r0, lw, (lane >> 3) * 1024 + col0 * 2 + (lane & 7) * 16);
    VM0();
    bf16x8 wf[2][4];
#pragma unroll
    for (int ks = 0; ks < 2; ++ks)
#pragma unroll
      for (int t = 0; t < 4; ++t) wf[ks][t] = trfrag(LW + (ks * 32 + (n >> 2) * 8 + g) * 128 + (t * 16 + (n & 3) * 4) * 2, 512);
    int ao = L_ACT + (n & 3) * APITCH + g * 16, so = WS_B + (c * 2048 + (n & 3) * 512 + col0 + (n >> 2) * 16 + g * 4) * 4;
    PIN(ao); PIN(so);
    BARRIER();
    f32x4 acc[4];
#pragma unroll
    for (int ks = 0; ks < 2; ++ks) {
      const bf16x8 bf = *(LAS bf16x8*)(L + ao + ks * 64);
#pragma unroll
      for (int t = 0; t < 4; ++t) acc[t] = mfma32(wf[ks][t], bf, ks ? acc[t] : f32x4{0.f, 0.f, 0.f, 0.f});
    }
    st4(rw, so, pick4(acc[0], acc[1], acc[2], acc[3], n), tag);
  } else if (role == 1) {
    const int k = ridx;
    {
      const rsrc_t r1 = __builtin_amdgcn_make_buffer_rsrc((void*)wg, 0, 512 * FF * 2, RSRC3);
      const rsrc_t r2 = __builtin_amdgcn_make_buffer_rsrc((void*)wu, 0, 512 * FF * 2, RSRC3);
      const rsrc_t r3 = __builtin_amdgcn_make_buffer_rsrc((void*)(wd + (size_t)k * 32 * 512), 0, 32768, RSRC3);
      const int vo = (hw * 128 + (lane >> 2)) * (FF * 2) + k * 64 + (lane & 3) * 16;
      dmac_run<0, 8, 0, 16 * FF * 2>(r1, lw, vo);
      dmac_run<0, 8, 8192, 16 * FF * 2>(r2, lw, vo);
      dma_run<0, 8, 16384, 4096>(r3, lw, g * 1024 + hw * 256 + n * 16);
    }
    asm volatile("s_waitcnt vmcnt(8)" ::: "memory");
    bf16x8 wf[4][4], df[8];
#pragma unroll
    for (int ks = 0; ks < 4; ++ks)
#pragma unroll
      for (int t = 0; t < 4; ++t) wf[ks][t] = trfrag(LW + (t >> 1) * 8192 + (ks * 32 + g * 8 + (n >> 2)) * 64 + ((t & 1) * 16 + (n & 3) * 4) * 2, 256);
    VM0();
#pragma unroll
    for (int t = 0; t < 8; ++t) df[t] = trfrag(LW + 16384 + (g * 8 + (n >> 2)) * 256 + (t * 16 + (n & 3) * 4) * 2, 1024);
    int ao = L_ACT + (n & 3) * APITCH + (hw * 128 + g * 8) * 2, ro = L_RED2 + hw * 1024 + (n & 3) * 256 + ((n >> 2) * 16 + g * 4) * 4, ri2 = L_RED2 + g * 256 + n * 8;
    int wo2 = L_AI + hw * 256 + g * 64 + n * 4, bo = L_AI + hw * 256 + (n & 3) * 64 + g * 16, so = WS_D + (k * 2048 + (n & 3) * 512 + hw * 128 + (n >> 2) * 16 + g * 4) * 4;
    PIN(ao); PIN(ro); PIN(ri2); PIN(wo2); PIN(bo); PIN(so);
    BARRIER();
    f32x4 acc[8];
    {
      bf16x8 bf[4];
#pragma unroll
      for (int ks = 0; ks < 4; ++ks) bf[ks] = *(LAS bf16x8*)(L + ao + ks * 64);
#pragma unroll
      for (int ks = 0; ks < 4; ++ks)
#pragma unroll
        for (int t = 0; t < 4; ++t) acc[t] = mfma32(wf[ks][t], bf[ks], ks ? acc[t] : f32x4{0.f, 0.f, 0.f, 0.f});
    }
    *(LAS f32x4*)(L + ro) = pick4(acc[0], acc[1], acc[2], acc[3], n);
    BARRIER();
    {
      f32x2 gp[4], up[4];
#pragma unroll
      for (int i = 0; i < 4; ++i) { gp[i] = *(const LAS f32x2*)(L + ri2 + i * 1024); up[i] = *(const LAS f32x2*)(L + ri2 + i * 1024 + 128); }
      const f32x2 gs = (gp[0] + gp[1]) + (gp[2] + gp[3]), us = (up[0] + up[1]) + (up[2] + up[3]);
      float a[2];
#pragma unroll
      for (int i = 0; i < 2; ++i) {
        const float z = gs[i] * (-2.302208198f - 0.1029432397f * gs[i] * gs[i]);
        const float sg = __builtin_amdgcn_rcpf(1.f + __builtin_amdgcn_exp2f(z));
        a[i] = (gs[i] * us[i]) * sg;
      }
      *(LAS unsigned*)(L + wo2) = cvtpk(a[0], a[1]);
    }
    LGKM0();
    {
      const bf16x8 bf = *(LAS bf16x8*)(L + bo);
#pragma unroll
      for (int t = 0; t < 8; ++t) acc[t] = mfma32(df[t], bf, f32x4{0.f, 0.f, 0.f, 0.f});
#pragma unroll
      for (int hf = 0; hf < 2; ++hf) st4(rw, so + hf * 256, pick4(acc[4 * hf], acc[4 * hf + 1], acc[4 * hf + 2], acc[4 * hf + 3], n), tag);
    }
  } else {
    const int u = ridx * 4 + hw;
    const int p = lane & 3, cs = lane >> 2;
    const int pos = u * 16 + p * 4 + (g & 1) * 2 + (g >> 1);
    const float xv = bflo(*(const GAS unsigned short*)(x + pos));
    const unsigned tg = (g & 1) ? ((g & 2) ? tag.t[3] : tag.t[2]) : ((g & 2) ? tag.t[1] : tag.t[0]);
    float h;
    {
      int off[4];
#pragma unroll
      for (int i = 0; i < 4; ++i) off[i] = WS_B + ((i * 16 + cs) * 2048 + u * 16 + p * 4) * 4;
      u32x4 v[4];
      gather<4, true>(rw, off, v, tag);
      const f32x4 sv = (dec4(v[0]) + dec4(v[1])) + (dec4(v[2]) + dec4(v[3]));
      h = tr32(tr16(sv[0], sv[2]), tr16(sv[1], sv[3]));
      h += dpp<0x124>(h); h += dpp<0x128>(h);
      h += xv;
      __builtin_amdgcn_raw_buffer_store_b32(enc(h, tg), rw, WS_C + ((cs & 3) * 2048 + pos) * 4, 0, AUX_SC1);
    }
    {
      int off[4];
#pragma unroll
      for (int i = 0; i < 4; ++i) off[i] = WS_D + ((i * 16 + (i == 3 ? (cs & 7) : cs)) * 2048 + u * 16 + p * 4) * 4;
      u32x4 v[4];
      gather<4, true>(rw, off, v, tag);
      if (cs >= 8) v[3] = u32x4{0u, 0u, 0u, 0u};
      const f32x4 sv = (dec4(v[0]) + dec4(v[1])) + (dec4(v[2]) + dec4(v[3]));
      float r = tr32(tr16(sv[0], sv[2]), tr16(sv[1], sv[3]));
      r += dpp<0x124>(r); r += dpp<0x128>(r);
      r += h;
      if ((cs & 3) == 0) *(GAS unsigned short*)(out + pos) = (unsigned short)cvtpk(r, r);
      if (u == 0) __builtin_amdgcn_raw_buffer_store_b64(u32x2{(unsigned)enext, (unsigned)(enext >> 32)}, rw, lane == 0 ? WS_E : OOB, 0, AUX_SC1);
    }
  }
}

extern "C" void kernel_launch(void* const* d_in, const int* in_sizes, int n_in, void* d_out, int out_size, void* d_ws, size_t ws_size,
                              hipStream_t stream) {
  (void)hipFuncSetAttribute((const void*)fused_decode_block, hipFuncAttributeMaxDynamicSharedMemorySize, L_END);
  fused_decode_block<<<256, 512, L_END, stream>>>((const unsigned short*)d_in[1], (const unsigned short*)d_in[2], (const unsigned short*)d_in[0],
                                                  (const unsigned short*)d_in[3], (unsigned char*)d_ws, (unsigned short*)d_out, (const unsigned short*)d_in[4],
                                                  (const unsigned short*)d_in[6], (const unsigned short*)d_in[7], (const unsigned short*)d_in[8]);
}
```

```cpp
#include <hip/hip_runtime.h>
#include <hip/hip_bf16.h>
#include <cstdint>

typedef unsigned u32x4 __attribute__((ext_vector_type(4)));
typedef unsigned u32x2 __attribute__((ext_vector_type(2)));
typedef float f32x4 __attribute__((ext_vector_type(4)));
typedef float f32x2 __attribute__((ext_vector_type(2)));
typedef short bf16x8 __attribute__((ext_vector_type(8)));
typedef short bf16x4 __attribute__((ext_vector_type(4)));
#define LAS __attribute__((address_space(3)))
#define GAS __attribute__((address_space(1)))
typedef __amdgpu_buffer_rsrc_t rsrc_t;

constexpr int SK = 16384, DH = 128, FF = 1792;
constexpr int NPK = 12;
constexpr int PACE_T = 5650;
constexpr int PACE_C = 35;
constexpr int PACE_CAP = 260;
constexpr int AUX_NT = 2, AUX_SC1 = 16, RSRC3 = 0x00020000;
constexpr unsigned SPIN_CAP = 1u << 16;
constexpr int OOB = 0x7ffffff0;

constexpr int WS_E = 0;
constexpr int WS_Q = 4096;
constexpr int WS_A = WS_Q + 16384 * 8;
constexpr int WS_ML = WS_A + 131072 * 4;
constexpr int WS_B = WS_ML + 1024 * 16;
constexpr int WS_C = WS_B + 131072 * 4;
constexpr int WS_D = WS_C + 4 * 2048 * 4;
constexpr int WS_END = WS_D + 56 * 2048 * 4;

constexpr int L_W = 0;
constexpr int L_WH = 24576;
constexpr int L_ACT = 4 * L_WH;
constexpr int APITCH = 1040;
constexpr int L_RED = L_ACT + 4 * APITCH;
constexpr int L_ML = L_RED + 8192;
constexpr int L_AI = L_ML + 128;
constexpr int L_QI = L_AI + 1024;
constexpr int L_RED2 = L_QI + 2048;
constexpr int L_QF = L_RED2 + 4096;
constexpr int L_FLAG = L_QF + 32;
constexpr int L_T1 = L_FLAG + 16;
constexpr int L_END = L_T1 + 16;

__device__ __forceinline__ unsigned cvtpk(float lo, float hi) {
  typedef __bf16 bf2_t __attribute__((ext_vector_type(2)));
  f32x2 v = {lo, hi}; return __builtin_bit_cast(unsigned, __builtin_convertvector(v, bf2_t));
}
__device__ __forceinline__ float bflo(unsigned u) { return __builtin_bit_cast(float, u << 16); }
__device__ __forceinline__ float bfhi(unsigned u) { return __builtin_bit_cast(float, u & 0xffff0000u); }
__device__ __forceinline__ float asf(unsigned u) { return __builtin_bit_cast(float, u); }
__device__ __forceinline__ unsigned asu(float f) { return __builtin_bit_cast(unsigned, f); }
template <int CTRL> __device__ __forceinline__ float dpp(float x) {
  return __builtin_bit_cast(float, __builtin_amdgcn_mov_dpp(__builtin_bit_cast(int, x), CTRL, 0xf, 0xf, true));
}
__device__ __forceinline__ float xg_max(float x) {
  auto s = __builtin_amdgcn_permlane16_swap(asu(x), asu(x), false, false);
  x = fmaxf(asf(s[0]), asf(s[1]));
  auto t = __builtin_amdgcn_permlane32_swap(asu(x), asu(x), false, false);
  return fmaxf(asf(t[0]), asf(t[1]));
}
__device__ __forceinline__ float tr16(float a, float b) { auto s = __builtin_amdgcn_permlane16_swap(asu(a), asu(b), false, false); return asf(s[0]) + asf(s[1]); }
__device__ __forceinline__ float tr32(float a, float b) { auto s = __builtin_amdgcn_permlane32_swap(asu(a), asu(b), false, false); return asf(s[0]) + asf(s[1]); }
__device__ __forceinline__ float xg_sum(float x) { x = tr16(x, x); return tr32(x, x); }
__device__ __forceinline__ float sum16(float x) { x += dpp<0x124>(x); x += dpp<0x128>(x); return xg_sum(x); }
__device__ __forceinline__ float wave_sum(float x) { x += dpp<0xB1>(x); x += dpp<0x4E>(x); return sum16(x); }
#define BARRIER() do { __builtin_amdgcn_sched_barrier(0); asm volatile("s_waitcnt lgkmcnt(0)\n\ts_barrier" ::: "memory"); __builtin_amdgcn_sched_barrier(0); } while (0)
#define LGKM0() asm volatile("s_waitcnt lgkmcnt(0)" ::: "memory")
#define VM0() asm volatile("s_waitcnt vmcnt(0)" ::: "memory")
#define PIN(x) asm volatile("" : "+v"(x))

__device__ __forceinline__ u32x4 ldpoll(rsrc_t rs, int off) {
  asm volatile("" : "+v"(off) :: "memory");
  return __builtin_amdgcn_raw_buffer_load_b128(rs, off, 0, AUX_SC1);
}
__device__ __forceinline__ unsigned ldsu(LAS unsigned char* p) { return __builtin_amdgcn_readfirstlane(*(volatile LAS unsigned*)p); }
struct Tag { unsigned t[4]; };
__device__ __forceinline__ unsigned enc(float v, unsigned t) { return ((asu(v) + 0x800u) & 0xfffff000u) | t; }
__device__ __forceinline__ float dec(unsigned u) { return asf(u & 0xfffff000u); }
__device__ __forceinline__ f32x4 dec4(u32x4 v) { return f32x4{dec(v[0]), dec(v[1]), dec(v[2]), dec(v[3])}; }
__device__ __forceinline__ void st4(rsrc_t rs, int off, f32x4 v, const Tag& g) {
  __builtin_amdgcn_raw_buffer_store_b128(u32x4{enc(v[0], g.t[0]), enc(v[1], g.t[1]), enc(v[2], g.t[2]), enc(v[3], g.t[3])}, rs, off, 0, AUX_SC1);
}
__device__ __forceinline__ unsigned bad4(u32x4 v, const Tag& g) { return (v[0] ^ g.t[0]) | (v[1] ^ g.t[1]) | (v[2] ^ g.t[2]) | (v[3] ^ g.t[3]); }
__device__ __forceinline__ Tag make_tag(unsigned long long e) {
  Tag g{{(unsigned)e & 0xfffu, (unsigned)(e >> 12) & 0xfffu, (unsigned)(e >> 24) & 0xfffu, (unsigned)(e >> 36) & 0xfffu}};
  asm volatile("" : "+v"(g.t[0]), "+v"(g.t[1]), "+v"(g.t[2]), "+v"(g.t[3]));
  return g;
}
template <int N, bool SENT> __device__ __forceinline__ void gather(rsrc_t rs, const int (&off)[N], u32x4 (&v)[N], const Tag& g) {
  if (SENT) {
#pragma nounroll
    for (unsigned spins = 0; spins < SPIN_CAP; ++spins) {
      const u32x4 s0 = ldpoll(rs, off[0]);
      if (__any((bad4(s0, g) & 0xfffu) == 0u)) break;
      __builtin_amdgcn_s_sleep(2);
    }
  }
  unsigned bad = 1u;
#pragma unroll
  for (int i = 0; i < N; ++i) v[i] = u32x4{0u, 0u, 0u, 0u};
#pragma nounroll
  for (unsigned spins = 0; spins < SPIN_CAP; ++spins) {
    if (bad) {
#pragma unroll
      for (int i = 0; i < N; ++i) v[i] = ldpoll(rs, off[i]);
      bad = 0u;
#pragma unroll
      for (int i = 0; i < N; ++i) bad |= bad4(v[i], g);
      bad &= 0xfffu;
    }
    if (__all(bad == 0u)) break;
  }
  asm volatile("" ::: "memory");
}
#define DMA16(NAME, POL) \
template <int LOFF, int SOFF> __device__ __forceinline__ void NAME(rsrc_t rs, unsigned lds, int voff) { \
  unsigned keep, so; \
  asm volatile("s_mov_b32 %0, m0\n\ts_add_u32 m0, %3, %5\n\ts_mov_b32 %1, %6\n\tbuffer_load_dwordx4 %2, %4, %1 offen " POL "lds\n\ts_mov_b32 m0, %0" \
               : "=&s"(keep), "=&s"(so) : "v"(voff), "s"(lds), "s"(rs), "i"(LOFF), "i"(SOFF) : "memory", "scc"); \
} \
template <int I, int N, int L0, int SSTR> __device__ __forceinline__ void NAME##_run(rsrc_t rs, unsigned lds, int voff) { \
  if constexpr (I < N) { NAME<L0 + I * 1024, I * SSTR>(rs, lds, voff); NAME##_run<I + 1, N, L0, SSTR>(rs, lds, voff); } \
}
DMA16(dma, "nt ")
DMA16(dmac, "")
__device__ __forceinline__ bf16x8 trfrag(LAS unsigned char* p, int second) {
  typedef short s4 __attribute__((ext_vector_type(4)));
  const s4 a = __builtin_amdgcn_ds_read_tr16_b64_v4i16((LAS s4*)p);
  const s4 b = __builtin_amdgcn_ds_read_tr16_b64_v4i16((LAS s4*)(p + second));
  return __builtin_shufflevector(a, b, 0, 1, 2, 3, 4, 5, 6, 7);
}
__device__ __forceinline__ f32x4 mfma32(bf16x8 a, bf16x8 b, f32x4 c) { return __builtin_amdgcn_mfma_f32_16x16x32_bf16(a, b, c, 0, 0, 0); }
__device__ __forceinline__ f32x4 pick4(f32x4 a0, f32x4 a1, f32x4 a2, f32x4 a3, int n) {
  f32x4 r;
#pragma unroll
  for (int i = 0; i < 4; ++i) r[i] = (n & 8) ? ((n & 4) ? a3[i] : a2[i]) : ((n & 4) ? a1[i] : a0[i]);
  return r;
}

__global__ void __launch_bounds__(512, 2) fused_decode_block(const unsigned short* __restrict__ kc, const unsigned short* __restrict__ vc, const unsigned short* __restrict__ x,
                                                             const unsigned short* __restrict__ wq, unsigned char* ws, unsigned short* out, const unsigned short* __restrict__ wo,
                                                             const unsigned short* __restrict__ wg, const unsigned short* __restrict__ wu, const unsigned short* __restrict__ wd) {
  extern __shared__ __attribute__((aligned(16))) unsigned char smem[];
  LAS unsigned char* L = (LAS unsigned char*)smem;
  const int tid = threadIdx.x, lane = tid & 63, w = __builtin_amdgcn_readfirstlane(tid >> 6), blk = blockIdx.x;
  const int g = lane >> 4, n = lane & 15;
  const int pair = blk >> 3, split = blk & 7;
  const int b = pair >> 3, hkv = pair & 7;
#define ROLE() \
  int pr_ = pair; asm volatile("" : "+s"(pr_)); \
  const int ri = (split + pr_) & 7; \
  const int ridx = ri < 4 ? pr_ * 2 + (ri & 1) : ri == 4 ? pr_ * 2 : ri == 5 ? ((pr_ + 31) & 31) * 2 + 1 : pr_; \
  const int role = ri < 4 ? 0 : ri < 6 ? (ridx < 56 ? 1 : 3) : ri == 6 ? 2 : 3;

  if (__builtin_expect(w < 4, 1)) {
    unsigned long long tst64;
    asm volatile("s_memrealtime %0" : "=s"(tst64));
    __builtin_amdgcn_sched_barrier(0);
    const rsrc_t rk = __builtin_amdgcn_make_buffer_rsrc((void*)(kc + (size_t)pair * SK * DH), 0, SK * 256, RSRC3);
    const rsrc_t rv = __builtin_amdgcn_make_buffer_rsrc((void*)(vc + (size_t)pair * SK * DH), 0, SK * 256, RSRC3);
    const int koff = (4 * (n & 3) + ((n >> 2) & 1)) * 256 + (n >> 3) * 64 + 16 * g, voff = lane * 16;
    const int t0 = split * 128 + w;
#define TOFF(t) ((t0 + (t) * 4) * 4096)
#define LOADG(q, i) ((q) < 32 ? __builtin_amdgcn_raw_buffer_load_b128(rk, koff + ((i) >> 1) * 512 + ((i) & 1) * 128, TOFF((q) & 31), AUX_NT) \
                              : __builtin_amdgcn_raw_buffer_load_b128(rv, voff + (i) * 1024, TOFF((q) & 31), AUX_NT))
#define SLOT(q) ((q) < NPK ? (q) : ((q) - NPK) & 3)
#define NEXT(q) ((q) < 4 ? (q) + NPK : (q) < NPK ? 64 : (q) + 4)
    unsigned t1 = 0u;
#define PACE(q) asm volatile("1:\n\ts_memrealtime s[98:99]\n\ts_waitcnt lgkmcnt(0)\n\ts_sub_u32 s98, s98, %0\n\ts_cmp_lt_i32 s98, %1\n\ts_cbranch_scc0 2f\n\ts_sleep 1\n\ts_branch 1b\n2:" \
                             :: "s"(t1), "i"(PACE_C + ((q) * PACE_T) / 100) : "scc", "memory", "s98", "s99")
    u32x4 R[NPK][4];
#pragma unroll
    for (int q = 0; q < NPK; ++q) {
      if (q == 2) { __builtin_amdgcn_s_waitcnt(0x0F77); t1 = (unsigned)__builtin_amdgcn_s_memrealtime(); __builtin_amdgcn_sched_barrier(0); }
#pragma unroll
      for (int i = 0; i < 4; ++i) R[q][i] = LOADG(q, i);
      __builtin_amdgcn_sched_barrier(0);
    }
    if (lane == 0) *(LAS unsigned*)(L + L_T1 + w * 4) = t1;
    BARRIER();
    {
      const u32x4 tt = *(LAS u32x4*)(L + L_T1);
      const int d = min(min((int)(tt[0] - t1), (int)(tt[1] - t1)), min((int)(tt[2] - t1), (int)(tt[3] - t1)));
      t1 = __builtin_amdgcn_readfirstlane(t1 + (unsigned)d);
      asm volatile("" : "+s"(tst64));
      t1 = (unsigned)tst64 + (unsigned)(min((int)(t1 - (unsigned)tst64), PACE_CAP) >> 1);
    }
    bf16x8 qf[2];
#pragma unroll
    for (int j = 0; j < 2; ++j) qf[j] = *(LAS bf16x8*)(L + L_QI + (n & 1) * 1024 + (n >> 2) * 256 + j * 128 + ((n >> 1) & 1) * 64 + g * 16);
    f32x4 S[32];
    float m = -INFINITY;
#pragma unroll
    for (int q = 0; q < 32; ++q) {
      f32x4 D[2];
      if (NEXT(q) < 64) PACE(NEXT(q));
#pragma unroll
      for (int i = 0; i < 4; ++i) {
        D[i >> 1] = mfma32(__builtin_bit_cast(bf16x8, R[SLOT(q)][i]), qf[i & 1], (i & 1) ? D[i >> 1] : f32x4{0.f, 0.f, 0.f, 0.f});
        __builtin_amdgcn_sched_barrier(0);
        if (NEXT(q) < 64) R[SLOT(q)][i] = LOADG(NEXT(q), i);
        __builtin_amdgcn_sched_barrier(0);
      }
#pragma unroll
      for (int i = 0; i < 4; ++i) {
        auto sw = __builtin_amdgcn_permlane32_swap(asu(D[0][i]), asu(D[1][i]), false, false);
        float z = (n & 2) ? asf(sw[1]) : asf(sw[0]);
        z += dpp<0xB1>(z); z += dpp<0x4E>(z);
        S[q][i] = z;
      }
      m = fmaxf(fmaxf(m, fmaxf(S[q][0], S[q][1])), fmaxf(S[q][2], S[q][3]));
      __builtin_amdgcn_sched_barrier(0);
    }
    m = xg_max(m);
    const Tag tag = make_tag(((unsigned long long)ldsu(L + L_QF + 4) << 32) | ldsu(L + L_QF));
    ROLE();
    const rsrc_t rw = __builtin_amdgcn_make_buffer_rsrc((void*)ws, 0, WS_END, RSRC3);
    const unsigned M0 = (n & 2) ? 0u : ((n & 1) ? 0xffff0000u : 0xffffu), M1 = (n & 2) ? ((n & 1) ? 0xffff0000u : 0xffffu) : 0u;
    f32x4 olo = {0.f, 0.f, 0.f, 0.f}, ohi = {0.f, 0.f, 0.f, 0.f};
    float l = 0.f;
#pragma unroll
    for (int q = 32; q < 64; ++q) {
      if (q == 60 && tid == 0) *(volatile LAS unsigned*)(L + L_FLAG + 4) = 1u;
      if (NEXT(q) < 64) PACE(NEXT(q));
#pragma unroll
      for (int i = 0; i < 4; ++i) {
        const float p = __builtin_amdgcn_exp2f(S[q - 32][i] - m);
        const unsigned pk = cvtpk(p, p);
        l += bfhi(pk);
        const u32x2 bb = {pk & M0, pk & M1};
        const u32x4 vv = R[SLOT(q)][i];
        const u32x2 alo = {vv[0], vv[1]}, ahi = {vv[2], vv[3]};
        olo = __builtin_amdgcn_mfma_f32_16x16x16bf16_1k(__builtin_bit_cast(bf16x4, alo), __builtin_bit_cast(bf16x4, bb), olo, 0, 0, 0);
        ohi = __builtin_amdgcn_mfma_f32_16x16x16bf16_1k(__builtin_bit_cast(bf16x4, ahi), __builtin_bit_cast(bf16x4, bb), ohi, 0, 0, 0);
        __builtin_amdgcn_sched_barrier(0);
        if (NEXT(q) < 64) R[SLOT(q)][i] = LOADG(NEXT(q), i);
        __builtin_amdgcn_sched_barrier(0);
      }
    }
    *(LAS f32x4*)(L + L_RED + (w * 64 + lane) * 32) = olo;
    *(LAS f32x4*)(L + L_RED + (w * 64 + lane) * 32 + 16) = ohi;
    l = xg_sum(l);
    if (lane < 16 && (n & 3) == 0) *(LAS f32x2*)(L + L_ML + ((n >> 2) * 4 + w) * 8) = f32x2{m, l};
    LGKM0();
    if (lane == 0) __hip_atomic_fetch_add((LAS unsigned*)(L + L_FLAG), 1u, __ATOMIC_RELAXED, __HIP_MEMORY_SCOPE_WORKGROUP);
    const int hq = hkv * 4 + w, j = lane & 31;
    int poff = lane < 32 ? WS_A + ((((hq * 2 + (j >> 4)) * 4 + b) * 8 + split) * 64 + (j & 15) * 4) * 4 : lane == 32 ? WS_ML + ((hq * 4 + b) * 8 + split) * 16 : OOB;
    int src = L_RED + ((j >> 3) * 16 + w * 4 + ((j >> 1) & 3)) * 32 + (j & 1) * 16;
    PIN(poff); PIN(src);
#pragma nounroll
    for (unsigned spins = 0; spins < (SPIN_CAP << 4); ++spins) { if (ldsu(L + L_FLAG) == 4u) break; }
    asm volatile("" ::: "memory");
    {
      const f32x4 ml0 = *(const LAS f32x4*)(L + L_ML + w * 32), ml1 = *(const LAS f32x4*)(L + L_ML + w * 32 + 16);
      f32x4 p[4];
#pragma unroll
      for (int i = 0; i < 4; ++i) p[i] = *(const LAS f32x4*)(L + src + i * 2048);
      const float M = fmaxf(fmaxf(ml0[0], ml0[2]), fmaxf(ml1[0], ml1[2]));
      const float s0 = __builtin_amdgcn_exp2f(ml0[0] - M), s1 = __builtin_amdgcn_exp2f(ml0[2] - M), s2 = __builtin_amdgcn_exp2f(ml1[0] - M), s3 = __builtin_amdgcn_exp2f(ml1[2] - M);
      f32x4 o = (s0 * p[0] + s1 * p[1]) + (s2 * p[2] + s3 * p[3]);
      const float Ls = (s0 * ml0[1] + s1 * ml0[3]) + (s2 * ml1[1] + s3 * ml1[3]);
      const float Mh = dec(enc(M, 0u));
      if (lane >= 32) o = f32x4{Mh, Ls, M - Mh, Ls};
      st4(rw, poff, o, tag);
    }
    if (role == 0) {
      const int c = ridx, sp = lane >> 3, p0 = (lane & 7) * 8;
      const int o0 = WS_A + (((c * 4 + w) * 8 + sp) * 64 + p0) * 4;
      const int off[3] = {o0, o0 + 16, WS_ML + (((c >> 1) * 4 + w) * 8 + sp) * 16};
      int ao = L_ACT + w * APITCH + (p0 + 4 * (g & 1) + 2 * (g >> 1)) * 2;
      PIN(ao);
      u32x4 v[3];
      gather<3, false>(rw, off, v, tag);
      const float Ms = dec(v[2][0]) + dec(v[2][2]);
      float M = fmaxf(Ms, dpp<0x128>(Ms)); M = xg_max(M);
      const float sc = __builtin_amdgcn_exp2f(Ms - M);
      float Ls = dec(v[2][1]) * sc; Ls += dpp<0x128>(Ls); Ls = xg_sum(Ls);
      const float f = sc * __builtin_amdgcn_rcpf(Ls);
      float a[4];
#pragma unroll
      for (int i = 0; i < 4; ++i) a[i] = tr16(dec(v[0][i]) * f, dec(v[1][i]) * f);
      float e0 = tr32(a[0], a[2]), e1 = tr32(a[1], a[3]);
      e0 += dpp<0x128>(e0); e1 += dpp<0x128>(e1);
      *(LAS unsigned*)(L + ao) = cvtpk(e0, e1);
      BARRIER();
    } else if (role == 1) {
      const int o0 = WS_C + ((ridx & 3) * 2048 + w * 512 + lane * 4) * 4;
      const int off[2] = {o0, o0 + 1024};
      u32x4 v[2];
      gather<2, true>(rw, off, v, tag);
      const float piv = dec(__builtin_amdgcn_readfirstlane(v[0][0]));
      float hv[8], sm = 0.f, sq = 0.f;
#pragma unroll
      for (int i = 0; i < 8; ++i) { hv[i] = dec(v[i >> 2][i & 3]) - piv; sm += hv[i]; sq += hv[i] * hv[i]; }
      sm = wave_sum(sm) * (1.f / 512.f); sq = wave_sum(sq) * (1.f / 512.f);
      const float rs = __builtin_amdgcn_rsqf(fmaxf(sq - sm * sm, 0.f) + 1e-5f);
#pragma unroll
      for (int i = 0; i < 2; ++i)
        *(LAS u32x2*)(L + L_ACT + w * APITCH + i * 512 + lane * 8) = u32x2{cvtpk((hv[4 * i] - sm) * rs, (hv[4 * i + 1] - sm) * rs), cvtpk((hv[4 * i + 2] - sm) * rs, (hv[4 * i + 3] - sm) * rs)};
      BARRIER();
    }
    return;
  }

  __builtin_amdgcn_s_setprio(2);
  unsigned long long eraw;
  asm volatile("s_load_dwordx2 %0, %1, 0x0" : "=s"(eraw) : "s"(ws) : "memory");
  const int hw = w - 4;
  const rsrc_t rw = __builtin_amdgcn_make_buffer_rsrc((void*)ws, 0, WS_END, RSRC3);
  unsigned long long enext;
  LAS unsigned char* LW = L + L_W + hw * L_WH;
  const int slice = ((split + hkv) & 7) * 4 + b;
  {
    const rsrc_t rx = __builtin_amdgcn_make_buffer_rsrc((void*)x, 0, 4096, RSRC3);
    const rsrc_t rq = __builtin_amdgcn_make_buffer_rsrc((void*)wq, 0, 512 * 4096 * 2, RSRC3);
    const u32x4 xf = __builtin_amdgcn_raw_buffer_load_b128(rx, (n & 3) * 1024 + (hw * 128 + (n >> 2) * 32 + g * 8) * 2, 0, 0);
    u32x4 wr[4];
#pragma unroll
    for (int i = 0; i < 4; ++i)
      wr[i] = __builtin_amdgcn_raw_buffer_load_b128(rq, (hw * 128 + (lane >> 1)) * 8192 + (hkv * 512 + slice * 16) * 2 + (lane & 1) * 16, i * 32 * 8192, AUX_NT);
    __builtin_amdgcn_sched_barrier(0);
    if (hw == 2 && lane < 2) *(LAS unsigned*)(L + L_FLAG + lane * 4) = 0u;
    float sm = 0.f;
#pragma unroll
    for (int j = 0; j < 4; ++j) sm += bflo(xf[j]) + bfhi(xf[j]);
    const float mu = sum16(sm) * (1.f / 128.f);
    float sq = 0.f;
#pragma unroll
    for (int j = 0; j < 4; ++j) { const float d0 = bflo(xf[j]) - mu, d1 = bfhi(xf[j]) - mu; sq += d0 * d0; sq += d1 * d1; }
    sq = sum16(sq);
    LAS float* rq2 = (LAS float*)(L + L_RED2 + hw * 512);
    if (lane < 4) { rq2[80 + lane] = mu; rq2[84 + lane] = sq; }
#pragma unroll
    for (int i = 0; i < 4; ++i) *(LAS u32x4*)(LW + i * 1024 + lane * 16) = wr[i];
    asm volatile("s_waitcnt lgkmcnt(0)" : "+s"(eraw) :: "memory");
    enext = eraw + 0x9E3779B97F4A7C15ull;
    const unsigned tlo = (unsigned)enext & 0xffu, thi = (unsigned)(enext >> 8);
    f32x4 qa = {0.f, 0.f, 0.f, 0.f}, qo = {0.f, 0.f, 0.f, 0.f};
    const u32x4 ones = {0x3f803f80u, 0x3f803f80u, 0x3f803f80u, 0x3f803f80u};
#pragma unroll
    for (int i = 0; i < 4; ++i) {
      const bf16x8 a = trfrag(LW + (i * 32 + g * 8 + (n >> 2)) * 32 + (n & 3) * 8, 128);
      const bool mine = (n >> 2) == i;
      const u32x4 xm = {mine ? xf[0] : 0u, mine ? xf[1] : 0u, mine ? xf[2] : 0u, mine ? xf[3] : 0u};
      qa = mfma32(a, __builtin_bit_cast(bf16x8, xm), qa);
      qo = mfma32(a, __builtin_bit_cast(bf16x8, ones), qo);
    }
#pragma unroll
    for (int i = 0; i < 4; ++i) { qa[i] += dpp<0x124>(qa[i]); qa[i] += dpp<0x128>(qa[i]); }
    if (n < 5) *(LAS f32x4*)(rq2 + n * 16 + g * 4) = n < 4 ? qa : qo;
    LGKM0();
    if (lane == 0) *(volatile LAS unsigned long long*)(L + L_QF + hw * 8) = enext;
    if (hw == 0) {
#pragma nounroll
      for (unsigned spins = 0; spins < (SPIN_CAP << 4); ++spins) {
        const unsigned long long f = *(volatile LAS unsigned long long*)(L + L_QF + (lane & 3) * 8);
        if (__all(f == enext)) break;
      }
      asm volatile("" ::: "memory");
      const int bb = (lane >> 3) & 3, c2 = (lane & 7) * 2;
      f32x2 dx = {0.f, 0.f}, d1 = {0.f, 0.f};
      float mq[4], msum = 0.f, m2 = 0.f;
#pragma unroll
      for (int i = 0; i < 4; ++i) {
        const LAS float* p = (const LAS float*)(L + L_RED2 + i * 512);
        dx += *(const LAS f32x2*)(p + bb * 16 + c2); d1 += *(const LAS f32x2*)(p + 64 + c2);
        mq[i] = p[80 + bb]; msum += mq[i]; m2 += p[84 + bb];
      }
      const float mean = msum * 0.25f;
#pragma unroll
      for (int i = 0; i < 4; ++i) m2 += 128.f * (mq[i] - mean) * (mq[i] - mean);
      const float rs = (0.08838834764831845f * 1.4426950408889634f) * __builtin_amdgcn_rsqf(m2 * (1.f / 512.f) + 1e-5f);
      const float q0 = (dx[0] - mean * d1[0]) * rs, q1 = (dx[1] - mean * d1[1]) * rs;
      const u32x4 gq = {((asu(q0) + 0x80u) & 0xffffff00u) | tlo, thi, ((asu(q1) + 0x80u) & 0xffffff00u) | tlo, thi};
      __builtin_amdgcn_raw_buffer_store_b128(gq, rw, lane < 32 ? WS_Q + ((hkv * 4 + bb) * 512 + slice * 16 + c2) * 8 : OOB, 0, AUX_SC1);
    }
    __builtin_amdgcn_s_sleep(40);
    const int t = tid - 256;
    u32x4 v = {0u, 0u, 0u, 0u};
    unsigned bad = 1u;
#pragma nounroll
    for (unsigned spins = 0; spins < SPIN_CAP; ++spins) {
      if (bad) { v = ldpoll(rw, WS_Q + ((hkv * 4 + b) * 512 + 2 * t) * 8); bad = (v[1] ^ thi) | (v[3] ^ thi) | (((v[0] ^ tlo) | (v[2] ^ tlo)) & 0xffu); }
      if (__all(bad == 0u)) break;
      __builtin_amdgcn_s_sleep(10);
    }
    const float q0 = asf(v[0] & 0xffffff00u), q1 = asf(v[2] & 0xffffff00u);
    const unsigned hi = cvtpk(q0, q1), lo = cvtpk(q0 - bflo(hi), q1 - bfhi(hi));
    *(LAS unsigned*)(L + L_QI + t * 4) = hi;
    *(LAS unsigned*)(L + L_QI + 1024 + t * 4) = lo;
  }
  BARRIER();
  if (tid == 511) {
    const unsigned t = __builtin_nontemporal_load((const GAS unsigned*)(vc + ((size_t)pair * SK + split * 2048) * DH));
    asm volatile("" :: "v"(t));
  }
  ROLE();
  if (role == 3) return;
  const Tag tag = make_tag(enext);
  __builtin_amdgcn_s_setprio(0);
  const unsigned lw = (unsigned)(L_W + hw * L_WH) + (unsigned)(size_t)L;
#pragma nounroll
  for (unsigned spins = 0; spins < (SPIN_CAP << 2); ++spins) {
    if (role == 0 ? ldsu(L + L_FLAG + 4) != 0u : ldsu(L + L_FLAG) == 4u) break;
    __builtin_amdgcn_s_sleep(4);
  }
  if (role == 0) {
    const int c = ridx, col0 = (ri >> 1) * 256 + hw * 64;
    const rsrc_t r0 = __builtin_amdgcn_make_buffer_rsrc((void*)(wo + (size_t)c * 64 * 512), 0, 65536, RSRC3);
    dma_run<0, 8, 0, 8192>(r0, lw, (lane >> 3) * 1024 + col0 * 2 + (lane & 7) * 16);
    VM0();
    bf16x8 wf[2][4];
#pragma unroll
    for (int ks = 0; ks < 2; ++ks)
#pragma unroll
      for (int t = 0; t < 4; ++t) wf[ks][t] = trfrag(LW + (ks * 32 + (n >> 2) * 8 + g) * 128 + (t * 16 + (n & 3) * 4) * 2, 512);
    int ao = L_ACT + (n & 3) * APITCH + g * 16, so = WS_B + (c * 2048 + (n & 3) * 512 + col0 + (n >> 2) * 16 + g * 4) * 4;
    PIN(ao); PIN(so);
    BARRIER();
    f32x4 acc[4];
#pragma unroll
    for (int ks = 0; ks < 2; ++ks) {
      const bf16x8 bf = *(LAS bf16x8*)(L + ao + ks * 64);
#pragma unroll
      for (int t = 0; t < 4; ++t) acc[t] = mfma32(wf[ks][t], bf, ks ? acc[t] : f32x4{0.f, 0.f, 0.f, 0.f});
    }
    st4(rw, so, pick4(acc[0], acc[1], acc[2], acc[3], n), tag);
  } else if (role == 1) {
    const int k = ridx;
    {
      const rsrc_t r1 = __builtin_amdgcn_make_buffer_rsrc((void*)wg, 0, 512 * FF * 2, RSRC3);
      const rsrc_t r2 = __builtin_amdgcn_make_buffer_rsrc((void*)wu, 0, 512 * FF * 2, RSRC3);
      const rsrc_t r3 = __builtin_amdgcn_make_buffer_rsrc((void*)(wd + (size_t)k * 32 * 512), 0, 32768, RSRC3);
      const int vo = (hw * 128 + (lane >> 2)) * (FF * 2) + k * 64 + (lane & 3) * 16;
      dmac_run<0, 8, 0, 16 * FF * 2>(r1, lw, vo);
      dmac_run<0, 8, 8192, 16 * FF * 2>(r2, lw, vo);
      dma_run<0, 8, 16384, 4096>(r3, lw, g * 1024 + hw * 256 + n * 16);
    }
    asm volatile("s_waitcnt vmcnt(8)" ::: "memory");
    bf16x8 wf[4][4], df[8];
#pragma unroll
    for (int ks = 0; ks < 4; ++ks)
#pragma unroll
      for (int t = 0; t < 4; ++t) wf[ks][t] = trfrag(LW + (t >> 1) * 8192 + (ks * 32 + g * 8 + (n >> 2)) * 64 + ((t & 1) * 16 + (n & 3) * 4) * 2, 256);
    VM0();
#pragma unroll
    for (int t = 0; t < 8; ++t) df[t] = trfrag(LW + 16384 + (g * 8 + (n >> 2)) * 256 + (t * 16 + (n & 3) * 4) * 2, 1024);
    int ao = L_ACT + (n & 3) * APITCH + (hw * 128 + g * 8) * 2, ro = L_RED2 + hw * 1024 + (n & 3) * 256 + ((n >> 2) * 16 + g * 4) * 4, ri2 = L_RED2 + g * 256 + n * 8;
    int wo2 = L_AI + hw * 256 + g * 64 + n * 4, bo = L_AI + hw * 256 + (n & 3) * 64 + g * 16, so = WS_D + (k * 2048 + (n & 3) * 512 + hw * 128 + (n >> 2) * 16 + g * 4) * 4;
    PIN(ao); PIN(ro); PIN(ri2); PIN(wo2); PIN(bo); PIN(so);
    BARRIER();
    f32x4 acc[8];
    {
      bf16x8 bf[4];
#pragma unroll
      for (int ks = 0; ks < 4; ++ks) bf[ks] = *(LAS bf16x8*)(L + ao + ks * 64);
#pragma unroll
      for (int ks = 0; ks < 4; ++ks)
#pragma unroll
        for (int t = 0; t < 4; ++t) acc[t] = mfma32(wf[ks][t], bf[ks], ks ? acc[t] : f32x4{0.f, 0.f, 0.f, 0.f});
    }
    *(LAS f32x4*)(L + ro) = pick4(acc[0], acc[1], acc[2], acc[3], n);
    BARRIER();
    {
      f32x2 gp[4], up[4];
#pragma unroll
      for (int i = 0; i < 4; ++i) { gp[i] = *(const LAS f32x2*)(L + ri2 + i * 1024); up[i] = *(const LAS f32x2*)(L + ri2 + i * 1024 + 128); }
      const f32x2 gs = (gp[0] + gp[1]) + (gp[2] + gp[3]), us = (up[0] + up[1]) + (up[2] + up[3]);
      float a[2];
#pragma unroll
      for (int i = 0; i < 2; ++i) {
        const float z = gs[i] * (-2.302208198f - 0.1029432397f * gs[i] * gs[i]);
        const float sg = __builtin_amdgcn_rcpf(1.f + __builtin_amdgcn_exp2f(z));
        a[i] = (gs[i] * us[i]) * sg;
      }
      *(LAS unsigned*)(L + wo2) = cvtpk(a[0], a[1]);
    }
    LGKM0();
    {
      const bf16x8 bf = *(LAS bf16x8*)(L + bo);
#pragma unroll
      for (int t = 0; t < 8; ++t) acc[t] = mfma32(df[t], bf, f32x4{0.f, 0.f, 0.f, 0.f});
#pragma unroll
      for (int hf = 0; hf < 2; ++hf) st4(rw, so + hf * 256, pick4(acc[4 * hf], acc[4 * hf + 1], acc[4 * hf + 2], acc[4 * hf + 3], n), tag);
    }
  } else {
    const int u = ridx * 4 + hw;
    const int p = lane & 3, cs = lane >> 2;
    const int pos = u * 16 + p * 4 + (g & 1) * 2 + (g >> 1);
    const float xv = bflo(*(const GAS unsigned short*)(x + pos));
    const unsigned tg = (g & 1) ? ((g & 2) ? tag.t[3] : tag.t[2]) : ((g & 2) ? tag.t[1] : tag.t[0]);
    float h;
    {
      int off[4];
#pragma unroll
      for (int i = 0; i < 4; ++i) off[i] = WS_B + ((i * 16 + cs) * 2048 + u * 16 + p * 4) * 4;
      u32x4 v[4];
      gather<4, true>(rw, off, v, tag);
      const f32x4 sv = (dec4(v[0]) + dec4(v[1])) + (dec4(v[2]) + dec4(v[3]));
      h = tr32(tr16(sv[0], sv[2]), tr16(sv[1], sv[3]));
      h += dpp<0x124>(h); h += dpp<0x128>(h);
      h += xv;
      __builtin_amdgcn_raw_buffer_store_b32(enc(h, tg), rw, WS_C + ((cs & 3) * 2048 + pos) * 4, 0, AUX_SC1);
    }
    {
      int off[4];
#pragma unroll
      for (int i = 0; i < 4; ++i) off[i] = WS_D + ((i * 16 + (i == 3 ? (cs & 7) : cs)) * 2048 + u * 16 + p * 4) * 4;
      u32x4 v[4];
      gather<4, true>(rw, off, v, tag);
      if (cs >= 8) v[3] = u32x4{0u, 0u, 0u, 0u};
      const f32x4 sv = (dec4(v[0]) + dec4(v[1])) + (dec4(v[2]) + dec4(v[3]));
      float r = tr32(tr16(sv[0], sv[2]), tr16(sv[1], sv[3]));
      r += dpp<0x124>(r); r += dpp<0x128>(r);
      r += h;
      if ((cs & 3) == 0) *(GAS unsigned short*)(out + pos) = (unsigned short)cvtpk(r, r);
      if (u == 0) __builtin_amdgcn_raw_buffer_store_b64(u32x2{(unsigned)enext, (unsigned)(enext >> 32)}, rw, lane == 0 ? WS_E : OOB, 0, AUX_SC1);
    }
  }
}

extern "C" void kernel_launch(void* const* d_in, const int* in_sizes, int n_in, void* d_out, int out_size, void* d_ws, size_t ws_size,
                              hipStream_t stream) {
  (void)hipFuncSetAttribute((const void*)fused_decode_block, hipFuncAttributeMaxDynamicSharedMemorySize, L_END);
  fused_decode_block<<<256, 512, L_END, stream>>>((const unsigned short*)d_in[1], (const unsigned short*)d_in[2], (const unsigned short*)d_in[0],
                                                  (const unsigned short*)d_in[3], (unsigned char*)d_ws, (unsigned short*)d_out, (const unsigned short*)d_in[4],
                                                  (const unsigned short*)d_in[6], (const unsigned short*)d_in[7], (const unsigned short*)d_in[8]);
}
```

```cpp
#include <hip/hip_runtime.h>
#include <hip/hip_bf16.h>
#include <cstdint>

typedef unsigned u32x4 __attribute__((ext_vector_type(4)));
typedef unsigned u32x2 __attribute__((ext_vector_type(2)));
typedef float f32x4 __attribute__((ext_vector_type(4)));
typedef float f32x2 __attribute__((ext_vector_type(2)));
typedef short bf16x8 __attribute__((ext_vector_type(8)));
typedef short bf16x4 __attribute__((ext_vector_type(4)));
#define LAS __attribute__((address_space(3)))
#define GAS __attribute__((address_space(1)))
typedef __amdgpu_buffer_rsrc_t rsrc_t;

constexpr int SK = 16384, DH = 128, FF = 1792;
constexpr int NPK = 12;
constexpr int PACE_T = 5650;
constexpr int PACE_C = 35;
constexpr int PACE_CAP = 260;
constexpr int AUX_NT = 2, AUX_SC1 = 16, RSRC3 = 0x00020000;
constexpr unsigned SPIN_CAP = 1u << 16;
constexpr int OOB = 0x7ffffff0;

constexpr int WS_E = 0;
constexpr int WS_Q = 4096;
constexpr int WS_A = WS_Q + 16384 * 8;
constexpr int WS_ML = WS_A + 131072 * 4;
constexpr int WS_B = WS_ML + 1024 * 16;
constexpr int WS_C = WS_B + 131072 * 4;
constexpr int WS_D = WS_C + 4 * 2048 * 4;
constexpr int WS_END = WS_D + 56 * 2048 * 4;

constexpr int L_W = 0;
constexpr int L_WH = 24576;
constexpr int L_ACT = 4 * L_WH;
constexpr int APITCH = 1040;
constexpr int L_RED = L_ACT + 4 * APITCH;
constexpr int L_ML = L_RED + 8192;
constexpr int L_AI = L_ML + 128;
constexpr int L_QI = L_AI + 1024;
constexpr int L_RED2 = L_QI + 2048;
constexpr int L_QF = L_RED2 + 4096;
constexpr int L_FLAG = L_QF + 32;
constexpr int L_T1 = L_FLAG + 16;
constexpr int L_END = L_T1 + 16;

__device__ __forceinline__ unsigned cvtpk(float lo, float hi) {
  typedef __bf16 bf2_t __attribute__((ext_vector_type(2)));
  f32x2 v = {lo, hi}; return __builtin_bit_cast(unsigned, __builtin_convertvector(v, bf2_t));
}
__device__ __forceinline__ float bflo(unsigned u) { return __builtin_bit_cast(float, u << 16); }
__device__ __forceinline__ float bfhi(unsigned u) { return __builtin_bit_cast(float, u & 0xffff0000u); }
__device__ __forceinline__ float asf(unsigned u) { return __builtin_bit_cast(float, u); }
__device__ __forceinline__ unsigned asu(float f) { return __builtin_bit_cast(unsigned, f); }
template <int CTRL> __device__ __forceinline__ float dpp(float x) {
  return __builtin_bit_cast(float, __builtin_amdgcn_mov_dpp(__builtin_bit_cast(int, x), CTRL, 0xf, 0xf, true));
}
__device__ __forceinline__ float xg_max(float x) {
  auto s = __builtin_amdgcn_permlane16_swap(asu(x), asu(x), false, false);
  x = fmaxf(asf(s[0]), asf(s[1]));
  auto t = __builtin_amdgcn_permlane32_swap(asu(x), asu(x), false, false);
  return fmaxf(asf(t[0]), asf(t[1]));
}
__device__ __forceinline__ float tr16(float a, float b) { auto s = __builtin_amdgcn_permlane16_swap(asu(a), asu(b), false, false); return asf(s[0]) + asf(s[1]); }
__device__ __forceinline__ float tr32(float a, float b) { auto s = __builtin_amdgcn_permlane32_swap(asu(a), asu(b), false, false); return asf(s[0]) + asf(s[1]); }
__device__ __forceinline__ float xg_sum(float x) { x = tr16(x, x); return tr32(x, x); }
__device__ __forceinline__ float sum16(float x) { x += dpp<0x124>(x); x += dpp<0x128>(x); return xg_sum(x); }
__device__ __forceinline__ float wave_sum(float x) { x += dpp<0xB1>(x); x += dpp<0x4E>(x); return sum16(x); }
#define BARRIER() do { __builtin_amdgcn_sched_barrier(0); asm volatile("s_waitcnt lgkmcnt(0)\n\ts_barrier" ::: "memory"); __builtin_amdgcn_sched_barrier(0); } while (0)
#define LGKM0() asm volatile("s_waitcnt lgkmcnt(0)" ::: "memory")
#define VM0() asm volatile("s_waitcnt vmcnt(0)" ::: "memory")
#define PIN(x) asm volatile("" : "+v"(x))

__device__ __forceinline__ u32x4 ldpoll(rsrc_t rs, int off) {
  asm volatile("" : "+v"(off) :: "memory");
  return __builtin_amdgcn_raw_buffer_load_b128(rs, off, 0, AUX_SC1);
}
__device__ __forceinline__ unsigned ldsu(LAS unsigned char* p) { return __builtin_amdgcn_readfirstlane(*(volatile LAS unsigned*)p); }
struct Tag { unsigned t[4]; };
__device__ __forceinline__ unsigned enc(float v, unsigned t) { return ((asu(v) + 0x800u) & 0xfffff000u) | t; }
__device__ __forceinline__ float dec(unsigned u) { return asf(u & 0xfffff000u); }
__device__ __forceinline__ f32x4 dec4(u32x4 v) { return f32x4{dec(v[0]), dec(v[1]), dec(v[2]), dec(v[3])}; }
__device__ __forceinline__ void st4(rsrc_t rs, int off, f32x4 v, const Tag& g) {
  __builtin_amdgcn_raw_buffer_store_b128(u32x4{enc(v[0], g.t[0]), enc(v[1], g.t[1]), enc(v[2], g.t[2]), enc(v[3], g.t[3])}, rs, off, 0, AUX_SC1);
}
__device__ __forceinline__ unsigned bad4(u32x4 v, const Tag& g) { return (v[0] ^ g.t[0]) | (v[1] ^ g.t[1]) | (v[2] ^ g.t[2]) | (v[3] ^ g.t[3]); }
__device__ __forceinline__ Tag make_tag(unsigned long long e) {
  Tag g{{(unsigned)e & 0xfffu, (unsigned)(e >> 12) & 0xfffu, (unsigned)(e >> 24) & 0xfffu, (unsigned)(e >> 36) & 0xfffu}};
  asm volatile("" : "+v"(g.t[0]), "+v"(g.t[1]), "+v"(g.t[2]), "+v"(g.t[3]));
  return g;
}
template <int N, bool SENT> __device__ __forceinline__ void gather(rsrc_t rs, const int (&off)[N], u32x4 (&v)[N], const Tag& g) {
  if (SENT) {
#pragma nounroll
    for (unsigned spins = 0; spins < SPIN_CAP; ++spins) {
      const u32x4 s0 = ldpoll(rs, off[0]);
      if (__any((bad4(s0, g) & 0xfffu) == 0u)) break;
      __builtin_amdgcn_s_sleep(2);
    }
  }
  unsigned bad = 1u;
#pragma unroll
  for (int i = 0; i < N; ++i) v[i] = u32x4{0u, 0u, 0u, 0u};
#pragma nounroll
  for (unsigned spins = 0; spins < SPIN_CAP; ++spins) {
    if (bad) {
#pragma unroll
      for (int i = 0; i < N; ++i) v[i] = ldpoll(rs, off[i]);
      bad = 0u;
#pragma unroll
      for (int i = 0; i < N; ++i) bad |= bad4(v[i], g);
      bad &= 0xfffu;
    }
    if (__all(bad == 0u)) break;
  }
  asm volatile("" ::: "memory");
}
#define DMA16(NAME, POL) \
template <int LOFF, int SOFF> __device__ __forceinline__ void NAME(rsrc_t rs, unsigned lds, int voff) { \
  unsigned keep, so; \
  asm volatile("s_mov_b32 %0, m0\n\ts_add_u32 m0, %3, %5\n\ts_mov_b32 %1, %6\n\tbuffer_load_dwordx4 %2, %4, %1 offen " POL "lds\n\ts_mov_b32 m0, %0" \
               : "=&s"(keep), "=&s"(so) : "v"(voff), "s"(lds), "s"(rs), "i"(LOFF), "i"(SOFF) : "memory", "scc"); \
} \
template <int I, int N, int L0, int SSTR> __device__ __forceinline__ void NAME##_run(rsrc_t rs, unsigned lds, int voff) { \
  if constexpr (I < N) { NAME<L0 + I * 1024, I * SSTR>(rs, lds, voff); NAME##_run<I + 1, N, L0, SSTR>(rs, lds, voff); } \
}
DMA16(dma, "nt ")
DMA16(dmac, "")
__device__ __forceinline__ bf16x8 trfrag(LAS unsigned char* p, int second) {
  typedef short s4 __attribute__((ext_vector_type(4)));
  const s4 a = __builtin_amdgcn_ds_read_tr16_b64_v4i16((LAS s4*)p);
  const s4 b = __builtin_amdgcn_ds_read_tr16_b64_v4i16((LAS s4*)(p + second));
  return __builtin_shufflevector(a, b, 0, 1, 2, 3, 4, 5, 6, 7);
}
__device__ __forceinline__ f32x4 mfma32(bf16x8 a, bf16x8 b, f32x4 c) { return __builtin_amdgcn_mfma_f32_16x16x32_bf16(a, b, c, 0, 0, 0); }
__device__ __forceinline__ f32x4 pick4(f32x4 a0, f32x4 a1, f32x4 a2, f32x4 a3, int n) {
  f32x4 r;
#pragma unroll
  for (int i = 0; i < 4; ++i) r[i] = (n & 8) ? ((n & 4) ? a3[i] : a2[i]) : ((n & 4) ? a1[i] : a0[i]);
  return r;
}

__global__ void __launch_bounds__(512, 2) fused_decode_block(const unsigned short* __restrict__ kc, const unsigned short* __restrict__ vc, const unsigned short* __restrict__ x,
                                                             const unsigned short* __restrict__ wq, unsigned char* ws, unsigned short* out, const unsigned short* __restrict__ wo,
                                                             const unsigned short* __restrict__ wg, const unsigned short* __restrict__ wu, const unsigned short* __restrict__ wd) {
  extern __shared__ __attribute__((aligned(16))) unsigned char smem[];
  LAS unsigned char* L = (LAS unsigned char*)smem;
  const int tid = threadIdx.x, lane = tid & 63, w = __builtin_amdgcn_readfirstlane(tid >> 6), blk = blockIdx.x;
  const int g = lane >> 4, n = lane & 15;
  const int pair = blk >> 3, split = blk & 7;
  const int b = pair >> 3, hkv = pair & 7;
#define ROLE() \
  int pr_ = pair; asm volatile("" : "+s"(pr_)); \
  const int ri = (split + pr_) & 7; \
  const int ridx = ri < 4 ? pr_ * 2 + (ri & 1) : ri == 4 ? pr_ * 2 : ri == 5 ? ((pr_ + 31) & 31) * 2 + 1 : pr_; \
  const int role = ri < 4 ? 0 : ri < 6 ? (ridx < 56 ? 1 : 3) : ri == 6 ? 2 : 3;

  if (__builtin_expect(w < 4, 1)) {
    unsigned long long tst64;
    asm volatile("s_memrealtime %0" : "=s"(tst64));
    __builtin_amdgcn_sched_barrier(0);
    const rsrc_t rk = __builtin_amdgcn_make_buffer_rsrc((void*)(kc + (size_t)pair * SK * DH), 0, SK * 256, RSRC3);
    const rsrc_t rv = __builtin_amdgcn_make_buffer_rsrc((void*)(vc + (size_t)pair * SK * DH), 0, SK * 256, RSRC3);
    const int koff = (4 * (n & 3) + ((n >> 2) & 1)) * 256 + (n >> 3) * 64 + 16 * g, voff = lane * 16;
    const int t0 = split * 128 + w;
#define TOFF(t) ((t0 + (t) * 4) * 4096)
#define LOADG(q, i) ((q) < 32 ? __builtin_amdgcn_raw_buffer_load_b128(rk, koff + ((i) >> 1) * 512 + ((i) & 1) * 128, TOFF((q) & 31), AUX_NT) \
                              : __builtin_amdgcn_raw_buffer_load_b128(rv, voff + (i) * 1024, TOFF((q) & 31), AUX_NT))
#define SLOT(q) ((q) < NPK ? (q) : ((q) - NPK) & 3)
#define NEXT(q) ((q) < 4 ? (q) + NPK : (q) < NPK ? 64 : (q) + 4)
    unsigned t1 = 0u;
#define PACE(q) asm volatile("1:\n\ts_memrealtime s[98:99]\n\ts_waitcnt lgkmcnt(0)\n\ts_sub_u32 s98, s98, %0\n\ts_cmp_lt_i32 s98, %1\n\ts_cbranch_scc0 2f\n\ts_sleep 1\n\ts_branch 1b\n2:" \
                             :: "s"(t1), "i"(PACE_C + ((q) * PACE_T) / 100) : "scc", "memory", "s98", "s99")
    u32x4 R[NPK][4];
#pragma unroll
    for (int q = 0; q < NPK; ++q) {
      if (q == 2) { __builtin_amdgcn_s_waitcnt(0x0F77); t1 = (unsigned)__builtin_amdgcn_s_memrealtime(); __builtin_amdgcn_sched_barrier(0); }
#pragma unroll
      for (int i = 0; i < 4; ++i) R[q][i] = LOADG(q, i);
      __builtin_amdgcn_sched_barrier(0);
    }
    if (lane == 0) *(LAS unsigned*)(L + L_T1 + w * 4) = t1;
    BARRIER();
    {
      const u32x4 tt = *(LAS u32x4*)(L + L_T1);
      const int d = min(min((int)(tt[0] - t1), (int)(tt[1] - t1)), min((int)(tt[2] - t1), (int)(tt[3] - t1)));
      t1 = __builtin_amdgcn_readfirstlane(t1 + (unsigned)d);
      asm volatile("" : "+s"(tst64));
      t1 = (unsigned)tst64 + (unsigned)(min((int)(t1 - (unsigned)tst64), PACE_CAP) >> 1);
    }
    bf16x8 qf[2];
#pragma unroll
    for (int j = 0; j < 2; ++j) qf[j] = *(LAS bf16x8*)(L + L_QI + (n & 1) * 1024 + (n >> 2) * 256 + j * 128 + ((n >> 1) & 1) * 64 + g * 16);
    f32x4 S[32];
    float m = -INFINITY;
#pragma unroll
    for (int q = 0; q < 32; ++q) {
      f32x4 D[2];
      if (NEXT(q) < 64) PACE(NEXT(q));
#pragma unroll
      for (int i = 0; i < 4; ++i) {
        D[i >> 1] = mfma32(__builtin_bit_cast(bf16x8, R[SLOT(q)][i]), qf[i & 1], (i & 1) ? D[i >> 1] : f32x4{0.f, 0.f, 0.f, 0.f});
        __builtin_amdgcn_sched_barrier(0);
        if (NEXT(q) < 64) R[SLOT(q)][i] = LOADG(NEXT(q), i);
        __builtin_amdgcn_sched_barrier(0);
      }
#pragma unroll
      for (int i = 0; i < 4; ++i) {
        auto sw = __builtin_amdgcn_permlane32_swap(asu(D[0][i]), asu(D[1][i]), false, false);
        float z = (n & 2) ? asf(sw[1]) : asf(sw[0]);
        z += dpp<0xB1>(z); z += dpp<0x4E>(z);
        S[q][i] = z;
      }
      m = fmaxf(fmaxf(m, fmaxf(S[q][0], S[q][1])), fmaxf(S[q][2], S[q][3]));
      __builtin_amdgcn_sched_barrier(0);
    }
    m = xg_max(m);
    const Tag tag = make_tag(((unsigned long long)ldsu(L + L_QF + 4) << 32) | ldsu(L + L_QF));
    ROLE();
    const rsrc_t rw = __builtin_amdgcn_make_buffer_rsrc((void*)ws, 0, WS_END, RSRC3);
    const unsigned M0 = (n & 2) ? 0u : ((n & 1) ? 0xffff0000u : 0xffffu), M1 = (n & 2) ? ((n & 1) ? 0xffff0000u : 0xffffu) : 0u;
    f32x4 olo = {0.f, 0.f, 0.f, 0.f}, ohi = {0.f, 0.f, 0.f, 0.f};
    float l = 0.f;
#pragma unroll
    for (int q = 32; q < 64; ++q) {
      if (q == 60 && tid == 0) *(volatile LAS unsigned*)(L + L_FLAG + 4) = 1u;
      if (NEXT(q) < 64) PACE(NEXT(q));
#pragma unroll
      for (int i = 0; i < 4; ++i) {
        const float p = __builtin_amdgcn_exp2f(S[q - 32][i] - m);
        const unsigned pk = cvtpk(p, p);
        l += bfhi(pk);
        const u32x2 bb = {pk & M0, pk & M1};
        const u32x4 vv = R[SLOT(q)][i];
        const u32x2 alo = {vv[0], vv[1]}, ahi = {vv[2], vv[3]};
        olo = __builtin_amdgcn_mfma_f32_16x16x16bf16_1k(__builtin_bit_cast(bf16x4, alo), __builtin_bit_cast(bf16x4, bb), olo, 0, 0, 0);
        ohi = __builtin_amdgcn_mfma_f32_16x16x16bf16_1k(__builtin_bit_cast(bf16x4, ahi), __builtin_bit_cast(bf16x4, bb), ohi, 0, 0, 0);
        __builtin_amdgcn_sched_barrier(0);
        if (NEXT(q) < 64) R[SLOT(q)][i] = LOADG(NEXT(q), i);
        __builtin_amdgcn_sched_barrier(0);
      }
    }
    *(LAS f32x4*)(L + L_RED + (w * 64 + lane) * 32) = olo;
    *(LAS f32x4*)(L + L_RED + (w * 64 + lane) * 32 + 16) = ohi;
    l = xg_sum(l);
    if (lane < 16 && (n & 3) == 0) *(LAS f32x2*)(L + L_ML + ((n >> 2) * 4 + w) * 8) = f32x2{m, l};
    LGKM0();
    if (lane == 0) __hip_atomic_fetch_add((LAS unsigned*)(L + L_FLAG), 1u, __ATOMIC_RELAXED, __HIP_MEMORY_SCOPE_WORKGROUP);
    const int hq = hkv * 4 + w, j = lane & 31;
    int poff = lane < 32 ? WS_A + ((((hq * 2 + (j >> 4)) * 4 + b) * 8 + split) * 64 + (j & 15) * 4) * 4 : lane == 32 ? WS_ML + ((hq * 4 + b) * 8 + split) * 16 : OOB;
    int src = L_RED + ((j >> 3) * 16 + w * 4 + ((j >> 1) & 3)) * 32 + (j & 1) * 16;
    PIN(poff); PIN(src);
#pragma nounroll
    for (unsigned spins = 0; spins < (SPIN_CAP << 4); ++spins) { if (ldsu(L + L_FLAG) == 4u) break; }
    asm volatile("" ::: "memory");
    {
      const f32x4 ml0 = *(const LAS f32x4*)(L + L_ML + w * 32), ml1 = *(const LAS f32x4*)(L + L_ML + w * 32 + 16);
      f32x4 p[4];
#pragma unroll
      for (int i = 0; i < 4; ++i) p[i] = *(const LAS f32x4*)(L + src + i * 2048);
      const float M = fmaxf(fmaxf(ml0[0], ml0[2]), fmaxf(ml1[0], ml1[2]));
      const float s0 = __builtin_amdgcn_exp2f(ml0[0] - M), s1 = __builtin_amdgcn_exp2f(ml0[2] - M), s2 = __builtin_amdgcn_exp2f(ml1[0] - M), s3 = __builtin_amdgcn_exp2f(ml1[2] - M);
      f32x4 o = (s0 * p[0] + s1 * p[1]) + (s2 * p[2] + s3 * p[3]);
      const float Ls = (s0 * ml0[1] + s1 * ml0[3]) + (s2 * ml1[1] + s3 * ml1[3]);
      const float Mh = dec(enc(M, 0u));
      if (lane >= 32) o = f32x4{Mh, Ls, M - Mh, Ls};
      st4(rw, poff, o, tag);
    }
    if (role == 0) {
      const int c = ridx, sp = lane >> 3, p0 = (lane & 7) * 8;
      const int o0 = WS_A + (((c * 4 + w) * 8 + sp) * 64 + p0) * 4;
      const int off[3] = {o0, o0 + 16, WS_ML + (((c >> 1) * 4 + w) * 8 + sp) * 16};
      int ao = L_ACT + w * APITCH + (p0 + 4 * (g & 1) + 2 * (g >> 1)) * 2;
      PIN(ao);
      u32x4 v[3];
      gather<3, false>(rw, off, v, tag);
      const float Ms = dec(v[2][0]) + dec(v[2][2]);
      float M = fmaxf(Ms, dpp<0x128>(Ms)); M = xg_max(M);
      const float sc = __builtin_amdgcn_exp2f(Ms - M);
      float Ls = dec(v[2][1]) * sc; Ls += dpp<0x128>(Ls); Ls = xg_sum(Ls);
      const float f = sc * __builtin_amdgcn_rcpf(Ls);
      float a[4];
#pragma unroll
      for (int i = 0; i < 4; ++i) a[i] = tr16(dec(v[0][i]) * f, dec(v[1][i]) * f);
      float e0 = tr32(a[0], a[2]), e1 = tr32(a[1], a[3]);
      e0 += dpp<0x128>(e0); e1 += dpp<0x128>(e1);
      *(LAS unsigned*)(L + ao) = cvtpk(e0, e1);
      BARRIER();
    } else if (role == 1) {
      const int o0 = WS_C + ((ridx & 3) * 2048 + w * 512 + lane * 4) * 4;
      const int off[2] = {o0, o0 + 1024};
      u32x4 v[2];
      gather<2, true>(rw, off, v, tag);
      const float piv = dec(__builtin_amdgcn_readfirstlane(v[0][0]));
      float hv[8], sm = 0.f, sq = 0.f;
#pragma unroll
      for (int i = 0; i < 8; ++i) { hv[i] = dec(v[i >> 2][i & 3]) - piv; sm += hv[i]; sq += hv[i] * hv[i]; }
      sm = wave_sum(sm) * (1.f / 512.f); sq = wave_sum(sq) * (1.f / 512.f);
      const float rs = __builtin_amdgcn_rsqf(fmaxf(sq - sm * sm, 0.f) + 1e-5f);
#pragma unroll
      for (int i = 0; i < 2; ++i)
        *(LAS u32x2*)(L + L_ACT + w * APITCH + i * 512 + lane * 8) = u32x2{cvtpk((hv[4 * i] - sm) * rs, (hv[4 * i + 1] - sm) * rs), cvtpk((hv[4 * i + 2] - sm) * rs, (hv[4 * i + 3] - sm) * rs)};
      BARRIER();
    }
    return;
  }

  __builtin_amdgcn_s_setprio(2);
  const int hw = w - 4;
  const rsrc_t rw = __builtin_amdgcn_make_buffer_rsrc((void*)ws, 0, WS_END, RSRC3);
  unsigned long long enext;
  LAS unsigned char* LW = L + L_W + hw * L_WH;
  const int slice = ((split + hkv) & 7) * 4 + b;
  {
    const rsrc_t rx = __builtin_amdgcn_make_buffer_rsrc((void*)x, 0, 4096, RSRC3);
    const rsrc_t rq = __builtin_amdgcn_make_buffer_rsrc((void*)wq, 0, 512 * 4096 * 2, RSRC3);
    const u32x4 xf = __builtin_amdgcn_raw_buffer_load_b128(rx, (n & 3) * 1024 + (hw * 128 + (n >> 2) * 32 + g * 8) * 2, 0, 0);
    u32x4 wr[4];
#pragma unroll
    for (int i = 0; i < 4; ++i)
      wr[i] = __builtin_amdgcn_raw_buffer_load_b128(rq, (hw * 128 + (lane >> 1)) * 8192 + (hkv * 512 + slice * 16) * 2 + (lane & 1) * 16, i * 32 * 8192, AUX_NT);
    const u32x2 ev = __builtin_amdgcn_raw_buffer_load_b64(rw, lane == 0 ? WS_E : OOB, 0, AUX_SC1);
    __builtin_amdgcn_sched_barrier(0);
    if (hw == 2 && lane < 2) *(LAS unsigned*)(L + L_FLAG + lane * 4) = 0u;
    float sm = 0.f;
#pragma unroll
    for (int j = 0; j < 4; ++j) sm += bflo(xf[j]) + bfhi(xf[j]);
    const float mu = sum16(sm) * (1.f / 128.f);
    float sq = 0.f;
#pragma unroll
    for (int j = 0; j < 4; ++j) { const float d0 = bflo(xf[j]) - mu, d1 = bfhi(xf[j]) - mu; sq += d0 * d0; sq += d1 * d1; }
    sq = sum16(sq);
    LAS float* rq2 = (LAS float*)(L + L_RED2 + hw * 512);
    if (lane < 4) { rq2[80 + lane] = mu; rq2[84 + lane] = sq; }
#pragma unroll
    for (int i = 0; i < 4; ++i) *(LAS u32x4*)(LW + i * 1024 + lane * 16) = wr[i];
    LGKM0();
    enext = (((unsigned long long)__builtin_amdgcn_readfirstlane(ev[1]) << 32) | __builtin_amdgcn_readfirstlane(ev[0])) + 0x9E3779B97F4A7C15ull;
    const unsigned tlo = (unsigned)enext & 0xffu, thi = (unsigned)(enext >> 8);
    f32x4 qa = {0.f, 0.f, 0.f, 0.f}, qo = {0.f, 0.f, 0.f, 0.f};
    const u32x4 ones = {0x3f803f80u, 0x3f803f80u, 0x3f803f80u, 0x3f803f80u};
#pragma unroll
    for (int i = 0; i < 4; ++i) {
      const bf16x8 a = trfrag(LW + (i * 32 + g * 8 + (n >> 2)) * 32 + (n & 3) * 8, 128);
      const bool mine = (n >> 2) == i;
      const u32x4 xm = {mine ? xf[0] : 0u, mine ? xf[1] : 0u, mine ? xf[2] : 0u, mine ? xf[3] : 0u};
      qa = mfma32(a, __builtin_bit_cast(bf16x8, xm), qa);
      qo = mfma32(a, __builtin_bit_cast(bf16x8, ones), qo);
    }
#pragma unroll
    for (int i = 0; i < 4; ++i) { qa[i] += dpp<0x124>(qa[i]); qa[i] += dpp<0x128>(qa[i]); }
    if (n < 5) *(LAS f32x4*)(rq2 + n * 16 + g * 4) = n < 4 ? qa : qo;
    LGKM0();
    if (lane == 0) *(volatile LAS unsigned long long*)(L + L_QF + hw * 8) = enext;
    if (hw == 0) {
#pragma nounroll
      for (unsigned spins = 0; spins < (SPIN_CAP << 4); ++spins) {
        const unsigned long long f = *(volatile LAS unsigned long long*)(L + L_QF + (lane & 3) * 8);
        if (__all(f == enext)) break;
      }
      asm volatile("" ::: "memory");
      const int bb = (lane >> 3) & 3, c2 = (lane & 7) * 2;
      f32x2 dx = {0.f, 0.f}, d1 = {0.f, 0.f};
      float mq[4], msum = 0.f, m2 = 0.f;
#pragma unroll
      for (int i = 0; i < 4; ++i) {
        const LAS float* p = (const LAS float*)(L + L_RED2 + i * 512);
        dx += *(const LAS f32x2*)(p + bb * 16 + c2); d1 += *(const LAS f32x2*)(p + 64 + c2);
        mq[i] = p[80 + bb]; msum += mq[i]; m2 += p[84 + bb];
      }
      const float mean = msum * 0.25f;
#pragma unroll
      for (int i = 0; i < 4; ++i) m2 += 128.f * (mq[i] - mean) * (mq[i] - mean);
      const float rs = (0.08838834764831845f * 1.4426950408889634f) * __builtin_amdgcn_rsqf(m2 * (1.f / 512.f) + 1e-5f);
      const float q0 = (dx[0] - mean * d1[0]) * rs, q1 = (dx[1] - mean * d1[1]) * rs;
      const u32x4 gq = {((asu(q0) + 0x80u) & 0xffffff00u) | tlo, thi, ((asu(q1) + 0x80u) & 0xffffff00u) | tlo, thi};
      __builtin_amdgcn_raw_buffer_store_b128(gq, rw, lane < 32 ? WS_Q + ((hkv * 4 + bb) * 512 + slice * 16 + c2) * 8 : OOB, 0, AUX_SC1);
    }
    __builtin_amdgcn_s_sleep(40);
    const int t = tid - 256;
    u32x4 v = {0u, 0u, 0u, 0u};
    unsigned bad = 1u;
#pragma nounroll
    for (unsigned spins = 0; spins < SPIN_CAP; ++spins) {
      if (bad) { v = ldpoll(rw, WS_Q + ((hkv * 4 + b) * 512 + 2 * t) * 8); bad = (v[1] ^ thi) | (v[3] ^ thi) | (((v[0] ^ tlo) | (v[2] ^ tlo)) & 0xffu); }
      if (__all(bad == 0u)) break;
      __builtin_amdgcn_s_sleep(10);
    }
    const float q0 = asf(v[0] & 0xffffff00u), q1 = asf(v[2] & 0xffffff00u);
    const unsigned hi = cvtpk(q0, q1), lo = cvtpk(q0 - bflo(hi), q1 - bfhi(hi));
    *(LAS unsigned*)(L + L_QI + t * 4) = hi;
    *(LAS unsigned*)(L + L_QI + 1024 + t * 4) = lo;
  }
  BARRIER();
  if (tid == 511) {
    const unsigned t = __builtin_nontemporal_load((const GAS unsigned*)(vc + ((size_t)pair * SK + split * 2048) * DH));
    asm volatile("" :: "v"(t));
  }
  ROLE();
  if (role == 3) return;
  const Tag tag = make_tag(enext);
  __builtin_amdgcn_s_setprio(0);
  const unsigned lw = (unsigned)(L_W + hw * L_WH) + (unsigned)(size_t)L;
#pragma nounroll
  for (unsigned spins = 0; spins < (SPIN_CAP << 2); ++spins) {
    if (role == 0 ? ldsu(L + L_FLAG + 4) != 0u : ldsu(L + L_FLAG) == 4u) break;
    __builtin_amdgcn_s_sleep(4);
  }
  if (role == 0) {
    const int c = ridx, col0 = (ri >> 1) * 256 + hw * 64;
    const rsrc_t r0 = __builtin_amdgcn_make_buffer_rsrc((void*)(wo + (size_t)c * 64 * 512), 0, 65536, RSRC3);
    dma_run<0, 8, 0, 8192>(r0, lw, (lane >> 3) * 1024 + col0 * 2 + (lane & 7) * 16);
    VM0();
    bf16x8 wf[2][4];
#pragma unroll
    for (int ks = 0; ks < 2; ++ks)
#pragma unroll
      for (int t = 0; t < 4; ++t) wf[ks][t] = trfrag(LW + (ks * 32 + (n >> 2) * 8 + g) * 128 + (t * 16 + (n & 3) * 4) * 2, 512);
    int ao = L_ACT + (n & 3) * APITCH + g * 16, so = WS_B + (c * 2048 + (n & 3) * 512 + col0 + (n >> 2) * 16 + g * 4) * 4;
    PIN(ao); PIN(so);
    BARRIER();
    f32x4 acc[4];
#pragma unroll
    for (int ks = 0; ks < 2; ++ks) {
      const bf16x8 bf = *(LAS bf16x8*)(L + ao + ks * 64);
#pragma unroll
      for (int t = 0; t < 4; ++t) acc[t] = mfma32(wf[ks][t], bf, ks ? acc[t] : f32x4{0.f, 0.f, 0.f, 0.f});
    }
    st4(rw, so, pick4(acc[0], acc[1], acc[2], acc[3], n), tag);
  } else if (role == 1) {
    const int k = ridx;
    {
      const rsrc_t r1 = __builtin_amdgcn_make_buffer_rsrc((void*)wg, 0, 512 * FF * 2, RSRC3);
      const rsrc_t r2 = __builtin_amdgcn_make_buffer_rsrc((void*)wu, 0, 512 * FF * 2, RSRC3);
      const rsrc_t r3 = __builtin_amdgcn_make_buffer_rsrc((void*)(wd + (size_t)k * 32 * 512), 0, 32768, RSRC3);
      const int vo = (hw * 128 + (lane >> 2)) * (FF * 2) + k * 64 + (lane & 3) * 16;
      dmac_run<0, 8, 0, 16 * FF * 2>(r1, lw, vo);
      dmac_run<0, 8, 8192, 16 * FF * 2>(r2, lw, vo);
      dma_run<0, 8, 16384, 4096>(r3, lw, g * 1024 + hw * 256 + n * 16);
    }
    asm volatile("s_waitcnt vmcnt(8)" ::: "memory");
    bf16x8 wf[4][4], df[8];
#pragma unroll
    for (int ks = 0; ks < 4; ++ks)
#pragma unroll
      for (int t = 0; t < 4; ++t) wf[ks][t] = trfrag(LW + (t >> 1) * 8192 + (ks * 32 + g * 8 + (n >> 2)) * 64 + ((t & 1) * 16 + (n & 3) * 4) * 2, 256);
    VM0();
#pragma unroll
    for (int t = 0; t < 8; ++t) df[t] = trfrag(LW + 16384 + (g * 8 + (n >> 2)) * 256 + (t * 16 + (n & 3) * 4) * 2, 1024);
    int ao = L_ACT + (n & 3) * APITCH + (hw * 128 + g * 8) * 2, ro = L_RED2 + hw * 1024 + (n & 3) * 256 + ((n >> 2) * 16 + g * 4) * 4, ri2 = L_RED2 + g * 256 + n * 8;
    int wo2 = L_AI + hw * 256 + g * 64 + n * 4, bo = L_AI + hw * 256 + (n & 3) * 64 + g * 16, so = WS_D + (k * 2048 + (n & 3) * 512 + hw * 128 + (n >> 2) * 16 + g * 4) * 4;
    PIN(ao); PIN(ro); PIN(ri2); PIN(wo2); PIN(bo); PIN(so);
    BARRIER();
    f32x4 acc[8];
    {
      bf16x8 bf[4];
#pragma unroll
      for (int ks = 0; ks < 4; ++ks) bf[ks] = *(LAS bf16x8*)(L + ao + ks * 64);
#pragma unroll
      for (int ks = 0; ks < 4; ++ks)
#pragma unroll
        for (int t = 0; t < 4; ++t) acc[t] = mfma32(wf[ks][t], bf[ks], ks ? acc[t] : f32x4{0.f, 0.f, 0.f, 0.f});
    }
    *(LAS f32x4*)(L + ro) = pick4(acc[0], acc[1], acc[2], acc[3], n);
    BARRIER();
    {
      f32x2 gp[4], up[4];
#pragma unroll
      for (int i = 0; i < 4; ++i) { gp[i] = *(const LAS f32x2*)(L + ri2 + i * 1024); up[i] = *(const LAS f32x2*)(L + ri2 + i * 1024 + 128); }
      const f32x2 gs = (gp[0] + gp[1]) + (gp[2] + gp[3]), us = (up[0] + up[1]) + (up[2] + up[3]);
      float a[2];
#pragma unroll
      for (int i = 0; i < 2; ++i) {
        const float z = gs[i] * (-2.302208198f - 0.1029432397f * gs[i] * gs[i]);
        const float sg = __builtin_amdgcn_rcpf(1.f + __builtin_amdgcn_exp2f(z));
        a[i] = (gs[i] * us[i]) * sg;
      }
      *(LAS unsigned*)(L + wo2) = cvtpk(a[0], a[1]);
    }
    LGKM0();
    {
      const bf16x8 bf = *(LAS bf16x8*)(L + bo);
#pragma unroll
      for (int t = 0; t < 8; ++t) acc[t] = mfma32(df[t], bf, f32x4{0.f, 0.f, 0.f, 0.f});
#pragma unroll
      for (int hf = 0; hf < 2; ++hf) st4(rw, so + hf * 256, pick4(acc[4 * hf], acc[4 * hf + 1], acc[4 * hf + 2], acc[4 * hf + 3], n), tag);
    }
  } else {
    const int u = ridx * 4 + hw;
    const int p = lane & 3, cs = lane >> 2;
    const int pos = u * 16 + p * 4 + (g & 1) * 2 + (g >> 1);
    const float xv = bflo(*(const GAS unsigned short*)(x + pos));
    const unsigned tg = (g & 1) ? ((g & 2) ? tag.t[3] : tag.t[2]) : ((g & 2) ? tag.t[1] : tag.t[0]);
    float h;
    {
      int off[4];
#pragma unroll
      for (int i = 0; i < 4; ++i) off[i] = WS_B + ((i * 16 + cs) * 2048 + u * 16 + p * 4) * 4;
      u32x4 v[4];
      gather<4, true>(rw, off, v, tag);
      const f32x4 sv = (dec4(v[0]) + dec4(v[1])) + (dec4(v[2]) + dec4(v[3]));
      h = tr32(tr16(sv[0], sv[2]), tr16(sv[1], sv[3]));
      h += dpp<0x124>(h); h += dpp<0x128>(h);
      h += xv;
      __builtin_amdgcn_raw_buffer_store_b32(enc(h, tg), rw, WS_C + ((cs & 3) * 2048 + pos) * 4, 0, AUX_SC1);
    }
    {
      int off[4];
#pragma unroll
      for (int i = 0; i < 4; ++i) off[i] = WS_D + ((i * 16 + (i == 3 ? (cs & 7) : cs)) * 2048 + u * 16 + p * 4) * 4;
      u32x4 v[4];
      gather<4, true>(rw, off, v, tag);
      if (cs >= 8) v[3] = u32x4{0u, 0u, 0u, 0u};
      const f32x4 sv = (dec4(v[0]) + dec4(v[1])) + (dec4(v[2]) + dec4(v[3]));
      float r = tr32(tr16(sv[0], sv[2]), tr16(sv[1], sv[3]));
      r += dpp<0x124>(r); r += dpp<0x128>(r);
      r += h;
      if ((cs & 3) == 0) *(GAS unsigned short*)(out + pos) = (unsigned short)cvtpk(r, r);
      if (u == 0) __builtin_amdgcn_raw_buffer_store_b64(u32x2{(unsigned)enext, (unsigned)(enext >> 32)}, rw, lane == 0 ? WS_E : OOB, 0, AUX_SC1);
    }
  }
}

extern "C" void kernel_launch(void* const* d_in, const int* in_sizes, int n_in, void* d_out, int out_size, void* d_ws, size_t ws_size,
                              hipStream_t stream) {
  (void)hipFuncSetAttribute((const void*)fused_decode_block, hipFuncAttributeMaxDynamicSharedMemorySize, L_END);
  fused_decode_block<<<256, 512, L_END, stream>>>((const unsigned short*)d_in[1], (const unsigned short*)d_in[2], (const unsigned short*)d_in[0],
                                                  (const unsigned short*)d_in[3], (unsigned char*)d_ws, (unsigned short*)d_out, (const unsigned short*)d_in[4],
                                                  (const unsigned short*)d_in[6], (const unsigned short*)d_in[7], (const unsigned short*)d_in[8]);
}
```
